```python
import torch

def _softmax(x, axis=-1):
    return torch.softmax(x, dim=axis)
def _gelu(x):
    return torch.nn.functional.gelu(x, approximate="tanh")
def _silu(x):
    return torch.nn.functional.silu(x)
def _sigmoid(x):
    return torch.sigmoid(x)
def _layernorm(x, axis=-1, eps=1e-5):
    m = x.mean(dim=axis, keepdim=True)
    v = x.var(dim=axis, keepdim=True, unbiased=False)
    return (x - m) / torch.sqrt(v + eps)
def _scatter_add(tgt, idx, src, axis=0):
    return tgt.clone().index_add_(axis, idx, src)
def _segment_sum(src, seg, n):
    out = torch.zeros((n,) + src.shape[1:], dtype=src.dtype)
    return out.index_add_(0, seg, src)
def _pad(x, pw):
    flat = tuple(v for pair in reversed(pw) for v in pair)
    return torch.nn.functional.pad(x, flat)

kernel_name = 'gated_mix__spectral_gfnet__spectral_gfnet__prompt'

def setup_inputs():

    _w = (lambda a: (lambda t: t.to(torch.bfloat16) if t.is_floating_point() else t)(torch.as_tensor(a)))
    def _A_setup():
        B, S, D = 1, 2048, 1024
        Df = D // 2 + 1
        return {
            "x": _w((lambda *s: torch.randn(*s, dtype=torch.bfloat16) * 0.05)(B, S, D)),
            "w_r": _w((lambda *s: torch.randn(*s, dtype=torch.bfloat16) * 0.05)(S, Df) * 0.02),
            "w_i": _w((lambda *s: torch.randn(*s, dtype=torch.bfloat16) * 0.05)(S, Df) * 0.02),
        }
    _A_in = _A_setup()

    def _B_setup():
        B, S, D = 1, 2048, 1024
        Df = D // 2 + 1
        return {
            "x": _w((lambda *s: torch.randn(*s, dtype=torch.bfloat16) * 0.05)(B, S, D)),
            "w_r": _w((lambda *s: torch.randn(*s, dtype=torch.bfloat16) * 0.05)(S, Df) * 0.02),
            "w_i": _w((lambda *s: torch.randn(*s, dtype=torch.bfloat16) * 0.05)(S, Df) * 0.02),
        }
    _B_in = _B_setup()

    return {
        "A_x": _A_in["x"],
        "A_w_r": _A_in["w_r"],
        "A_w_i": _A_in["w_i"],
        "B_x": _B_in["x"],
        "B_w_r": _B_in["w_r"],
        "B_w_i": _B_in["w_i"],
        "gate_w": _w((lambda *s: torch.randn(*s, dtype=torch.bfloat16) * 0.05)(1024) * 16.0),
    }

def reference(A_x, A_w_r, A_w_i, B_x, B_w_r, B_w_i, gate_w):
    def _A_ref(x, w_r, w_i):
        B, S, D = x.shape
        X = (lambda x, axes=(-2,-1): torch.fft.rfft2(x.float(), dim=axes))(x, axes=(-2, -1))
        W = w_r + 1j * w_i
        X = X * W
        y = (lambda x, s, axes=(-2,-1): torch.fft.irfft2(x, s=s, dim=axes).to(torch.bfloat16))(X, (S, D), axes=(-2, -1))
        return y

    def _B_ref(x, w_r, w_i):
        B, S, D = x.shape
        X = (lambda x, axes=(-2,-1): torch.fft.rfft2(x.float(), dim=axes))(x, axes=(-2, -1))
        W = w_r + 1j * w_i
        X = X * W
        y = (lambda x, s, axes=(-2,-1): torch.fft.irfft2(x, s=s, dim=axes).to(torch.bfloat16))(X, (S, D), axes=(-2, -1))
        return y

    x = A_x
    g = _sigmoid((x * gate_w).sum(-1, keepdims=True))
    a = _A_ref(x, A_w_r, A_w_i)
    b = _B_ref(x, B_w_r, B_w_i)
    return g * a + (1 - g) * b
```

```cpp
#include <hip/hip_runtime.h>
#include <hip/hip_bf16.h>
#include <stdint.h>

typedef unsigned short u16;
typedef unsigned int u32;
typedef __attribute__((ext_vector_type(8))) __bf16 bf16x8;
typedef __attribute__((ext_vector_type(2))) __bf16 bf16x2;
typedef __attribute__((ext_vector_type(4))) short s16x4;
typedef __attribute__((ext_vector_type(4))) float f32x4;
typedef __attribute__((ext_vector_type(2))) float f32x2;
typedef __attribute__((ext_vector_type(4))) unsigned u32x4;
typedef __attribute__((ext_vector_type(2))) unsigned u32x2;
#define LDSP __attribute__((address_space(3)))
#define GLBP __attribute__((address_space(1)))
#define DI __device__ __forceinline__

struct __attribute__((packed, aligned(2))) U4u { u32x4 v; };

DI u32 pk(float lo, float hi) {
    f32x2 t = {lo, hi};
    return __builtin_bit_cast(u32, __builtin_convertvector(t, bf16x2));
}
DI float bfr(float v) { return (float)(__bf16)v; }
DI f32x4 mma(u32x4 a, u32x4 b, f32x4 c) {
    return __builtin_amdgcn_mfma_f32_16x16x32_bf16(__builtin_bit_cast(bf16x8, a), __builtin_bit_cast(bf16x8, b), c, 0, 0, 0);
}
DI f32x4 mma16(u32x2 a, u32x2 b, f32x4 c) {
    return __builtin_amdgcn_mfma_f32_16x16x16bf16_1k(__builtin_bit_cast(s16x4, a), __builtin_bit_cast(s16x4, b), c, 0, 0, 0);
}
DI float dot2(u32 a, u32 b, float c) {
    return __builtin_amdgcn_fdot2_f32_bf16(__builtin_bit_cast(bf16x2, a), __builtin_bit_cast(bf16x2, b), c, false);
}
DI float cosr(float x) { return __builtin_amdgcn_cosf(x); }
DI float sinr(float x) { return __builtin_amdgcn_sinf(x); }
DI float lo16(u32 v) { return __builtin_bit_cast(float, v << 16); }
DI float hi16(u32 v) { return __builtin_bit_cast(float, v & 0xffff0000u); }

static constexpr int I2_ROW = 32768 + 128;
static constexpr size_t OFF_I1 = 0;
static constexpr size_t OFF_I2 = 4u << 20;
static constexpr size_t OFF_I1X = 13u << 20;
static constexpr size_t OFF_I2X = OFF_I1X + 8192;
static constexpr size_t OFF_G = OFF_I2X + 16384;
static constexpr size_t OFF_WSP = OFF_G + 16384;

static constexpr int ZP = 33 * 128;

__global__ __launch_bounds__(512) void k1(const u16* __restrict__ x, const u16* __restrict__ gw,
                                          const u16* __restrict__ w0, const u16* __restrict__ w1,
                                          const u16* __restrict__ w2, const u16* __restrict__ w3,
                                          char* __restrict__ ws) {
    __shared__ __attribute__((aligned(16))) char sm[8 * 4096 + 4 * ZP];
    const int tid = threadIdx.x, lane = tid & 63;
    const int wv = __builtin_amdgcn_readfirstlane(tid >> 6);
    const int p = wv >> 1, h = wv & 1;
    const int q = lane >> 4, n = lane & 15;
    const int id = blockIdx.x;
    const int s1 = (id & 7) * 32 + (id >> 3);
    char* xs = sm + wv * 4096;
    char* zs = sm + 8 * 4096;

    const int gch = lane ^ (((lane >> 4) & 1) << 1);
    {
        const u16* r0 = x + (size_t)(s1 + 256 * p) * 1024 + gch * 8;
        const u16* r1 = r0 + (size_t)1024 * 1024;
        __builtin_amdgcn_global_load_lds((const GLBP void*)r0, (LDSP void*)xs, 16, 0, 0);
        __builtin_amdgcn_global_load_lds((const GLBP void*)r0, (LDSP void*)xs, 16, 1024, 0);
        __builtin_amdgcn_global_load_lds((const GLBP void*)r1, (LDSP void*)(xs + 2048), 16, 0, 0);
        __builtin_amdgcn_global_load_lds((const GLBP void*)r1, (LDSP void*)(xs + 2048), 16, 1024, 0);
    }
    const u32x4 gw0 = *(const u32x4*)(gw + gch * 8);
    const u32x4 gw1 = *(const u32x4*)(gw + 512 + gch * 8);
    u16 wtouch = 0;
    const int ra = tid >> 4, ln = tid & 15;
    const int tp_ = 8 * (id >> 3) + (ra >> 2);
    if (ln < 9) {
        const int arr = ra & 3;
        const u16* wp = arr == 0 ? w0 : arr == 1 ? w1 : arr == 2 ? w2 : w3;
        wtouch = wp[(size_t)((id & 7) + 8 * tp_) * 513 + ln * 64];
    }

    float c4[4], s4[4];
#pragma unroll
    for (int i = 0; i < 4; ++i) {
        const float a = (float)((4 * q + i) * n) * (1.0f / 32.0f);
        c4[i] = cosr(a); s4[i] = sinr(a);
    }
    const u32 c01 = pk(c4[0], c4[1]), c23 = pk(c4[2], c4[3]), s01 = pk(s4[0], s4[1]), s23 = pk(s4[2], s4[3]);
    const u32 mn = (n & 1) ? 0x80008000u : 0u;
    const u32 mh = h ? 0x80000000u : 0u;
    const u32x4 Cf = {c01 ^ mh, c23 ^ mh, c01 ^ mh ^ mn, c23 ^ mh ^ mn};
    const u32x4 Sf = {s01 ^ mh, s23 ^ mh, s01 ^ mh ^ mn, s23 ^ mh ^ mn};
    const u32x4 Nf = Sf ^ 0x80008000u;
    float tc[2][4], ts[2][4];
    const int rho = 16 * h + n;
#pragma unroll
    for (int T = 0; T < 2; ++T)
#pragma unroll
        for (int i = 0; i < 4; ++i) {
            const float a = (float)((16 * T + 4 * q + i) * rho) * (1.0f / 1024.0f);
            tc[T][i] = cosr(a); ts[T][i] = sinr(a);
        }
    u32x2 SepA;
    {
        const int r = n >> 1, part = n & 1, sg = (r & 1) ? -1 : 1;
        const float aa = (float)(q * r + sg) * 0.125f, ab = (float)(q * r - sg) * 0.125f;
        const float R2 = 1.41421356237f;
        const float ar = R2 * cosr(aa), ai = -R2 * sinr(aa), br = R2 * cosr(ab), bi = -R2 * sinr(ab);
        SepA[0] = part ? pk(ai, ar) : pk(ar, -ai);
        SepA[1] = part ? pk(bi, -br) : pk(br, bi);
    }

    asm volatile("s_waitcnt vmcnt(0)" ::: "memory");
    f32x4 Dr[2], Di[2];
    const f32x4 z4 = {0.f, 0.f, 0.f, 0.f};
#pragma unroll
    for (int T = 0; T < 2; ++T) {
        const char* b = xs + 64 * (4 * q + (n >> 2)) + 32 * (T ^ (q & 1)) + 8 * (n & 3);
        u32x4 xa[2];
#pragma unroll
        for (int part = 0; part < 2; ++part) {
            const u32x2 a0 = __builtin_bit_cast(u32x2, __builtin_amdgcn_ds_read_tr16_b64_v4i16((LDSP s16x4*)(b + 2048 * part)));
            const u32x2 a1 = __builtin_bit_cast(u32x2, __builtin_amdgcn_ds_read_tr16_b64_v4i16((LDSP s16x4*)(b + 2048 * part + 1024)));
            xa[part] = u32x4{a0[0], a0[1], a1[0], a1[1]};
        }
        Dr[T] = mma(xa[0], Cf, z4); Dr[T] = mma(xa[1], Sf, Dr[T]);
        Di[T] = mma(xa[0], Nf, z4); Di[T] = mma(xa[1], Cf, Di[T]);
    }
    u32x4 Bd[2];
#pragma unroll
    for (int T = 0; T < 2; ++T) {
        float re[4], im[4];
#pragma unroll
        for (int i = 0; i < 4; ++i) {
            re[i] = Dr[T][i] * tc[T][i] + Di[T][i] * ts[T][i];
            im[i] = Di[T][i] * tc[T][i] - Dr[T][i] * ts[T][i];
        }
        Bd[T] = u32x4{pk(re[0], re[1]), pk(re[2], re[3]), pk(im[0], im[1]), pk(im[2], im[3])};
    }
    char* zrow = zs + p * ZP;
#pragma unroll
    for (int Tp = 0; Tp < 2; ++Tp) {
        f32x4 Zr = z4, Zi = z4;
#pragma unroll
        for (int T = 0; T < 2; ++T) {
            const u32 m = (Tp ? 0x80000000u : 0u) ^ (T ? mn : 0u);
            const u32x4 Are = {c01 ^ m, c23 ^ m, s01 ^ m, s23 ^ m};
            const u32x4 Aim = {s01 ^ m ^ 0x80008000u, s23 ^ m ^ 0x80008000u, c01 ^ m, c23 ^ m};
            Zr = mma(Are, Bd[T], Zr);
            Zi = mma(Aim, Bd[T], Zi);
        }
        const u32x4 o = {pk(Zr[0], Zi[0]), pk(Zr[1], Zi[1]), pk(Zr[2], Zi[2]), pk(Zr[3], Zi[3])};
        *(u32x4*)(zrow + rho * 128 + (((4 * Tp + q) ^ (rho & 7)) << 4)) = o;
        if (rho == 0) {
#pragma unroll
            for (int i = 0; i < 4; ++i)
                *(u32*)(zrow + 32 * 128 + (((16 * Tp + 4 * q + i - 1) & 31) << 2)) = o[i];
        }
    }
    {
        const u32x4 x0 = *(const u32x4*)(xs + 2048 * h + lane * 16);
        const u32x4 x1 = *(const u32x4*)(xs + 2048 * h + 1024 + lane * 16);
        float acc = 0.f;
#pragma unroll
        for (int i = 0; i < 4; ++i) {
            acc = dot2(x0[i], gw0[i], acc);
            acc = dot2(x1[i], gw1[i], acc);
        }
#pragma unroll
        for (int o = 32; o > 0; o >>= 1) acc += __shfl_xor(acc, o);
        const float z = bfr(acc);
        const float g = bfr(1.0f / (1.0f + __expf(-z)));
        const float og = bfr(1.0f - g);
        if (lane == 0) {
            float* G = (float*)(ws + OFF_G) + (s1 * 4 + p) * 4;
            G[h] = g; G[2 + h] = og;
        }
    }
    __syncthreads();
    {
        const int hh = wv & 1, m = wv >> 1;
        const int rr = 16 * hh + n, rm = 32 - rr;
        const char* zq = zs + q * ZP;
        const u32x4 dv = *(const u32x4*)(zq + rr * 128 + ((m ^ (rr & 7)) << 4));
        const u32x4 mv = *(const u32x4*)(zq + rm * 128 + (((7 - m) ^ (rm & 7)) << 4));
        u32* I1 = (u32*)(ws + OFF_I1);
#pragma unroll
        for (int e = 0; e < 4; ++e) {
            const f32x4 d = mma16(SepA, u32x2{dv[e], mv[3 - e]}, z4);
            const int c = hh + 2 * (4 * m + e);
            u32* o = I1 + ((size_t)((2 * q) * 32 + c) * 256 + s1) * 16 + n;
            o[0] = pk(d[0], d[1]);
            o[32 * 256 * 16] = pk(d[2], d[3]);
        }
        if (wv == 7) {
            const u32 v = *(const u32*)(zq + 64);
            const f32x4 d = mma16(SepA, u32x2{v, v}, z4);
            if (n == 0) {
                u32* o = (u32*)(ws + OFF_I1X) + (2 * q) * 256 + (s1 & 15) * 16 + (s1 >> 4);
                o[0] = pk(d[0], d[1]);
                o[256] = pk(d[2], d[3]);
            }
        }
    }
    if (ln == 8) ((u16*)(ws + OFF_WSP))[((id & 7) * 256 + tp_) * 4 + (ra & 3)] = wtouch;
}

struct K2C {
    u32x4 F1r, F1i, F2r, F2i, G1r, G1i, G2r, G2i;
    float t1c[4], t1s[4], t2c[4], t2s[4];
};
DI void k2_consts(K2C& k, int r, int q, int n) {
#pragma unroll
    for (int i = 0; i < 4; ++i) {
        const int e = 4 * q + i;
        const float a1 = (float)(e * (r + 8 * n));
        k.t1c[i] = cosr(a1 * (1.0f / 2048.0f)); k.t1s[i] = sinr(a1 * (1.0f / 2048.0f));
        float c = cosr(a1 * (1.0f / 128.0f)), s = sinr(a1 * (1.0f / 128.0f));
        k.F1r[i] = pk(c, s); k.F1i[i] = pk(-s, c);
        const float a2 = (float)(e * n) * (1.0f / 16.0f);
        c = cosr(a2); s = sinr(a2);
        k.F2r[i] = pk(c, s); k.F2i[i] = pk(-s, c);
        k.G1r[i] = pk(c, -s); k.G1i[i] = pk(s, c);
        const float a3 = (float)(n * (r + 8 * e));
        k.t2c[i] = cosr(a3 * (1.0f / 2048.0f)); k.t2s[i] = sinr(a3 * (1.0f / 2048.0f));
        c = cosr(a3 * (1.0f / 128.0f)); s = sinr(a3 * (1.0f / 128.0f));
        k.G2r[i] = pk(c, -s); k.G2i[i] = pk(s, c);
    }
}
DI void k2_fwd(const K2C& k, u32x4 a, f32x4& Xr, f32x4& Xi) {
    const f32x4 z4 = {0.f, 0.f, 0.f, 0.f};
    const f32x4 dr = mma(a, k.F1r, z4), di = mma(a, k.F1i, z4);
    u32x4 b;
#pragma unroll
    for (int i = 0; i < 4; ++i)
        b[i] = pk(dr[i] * k.t1c[i] + di[i] * k.t1s[i], di[i] * k.t1c[i] - dr[i] * k.t1s[i]);
    Xr = mma(k.F2r, b, z4); Xi = mma(k.F2i, b, z4);
}
DI u32x4 k2_inv(const K2C& k, f32x4 Xr, f32x4 Xi, const float* wr, const float* wi) {
    const f32x4 z4 = {0.f, 0.f, 0.f, 0.f};
    u32x4 y;
#pragma unroll
    for (int i = 0; i < 4; ++i)
        y[i] = pk(Xr[i] * wr[i] - Xi[i] * wi[i], Xr[i] * wi[i] + Xi[i] * wr[i]);
    const f32x4 er = mma(y, k.G1r, z4), ei = mma(y, k.G1i, z4);
    u32x4 b;
#pragma unroll
    for (int i = 0; i < 4; ++i)
        b[i] = pk(er[i] * k.t2c[i] - ei[i] * k.t2s[i], ei[i] * k.t2c[i] + er[i] * k.t2s[i]);
    const f32x4 orr = mma(k.G2r, b, z4), oi = mma(k.G2i, b, z4);
    u32x4 o;
#pragma unroll
    for (int i = 0; i < 4; ++i) o[i] = pk(orr[i], oi[i]);
    return o;
}

__global__ __launch_bounds__(256) void k2(const u16* __restrict__ w0, const u16* __restrict__ w1,
                                          const u16* __restrict__ w2, const u16* __restrict__ w3,
                                          char* __restrict__ ws) {
    __shared__ __attribute__((aligned(16))) char sm[65536];
    const int tid = threadIdx.x, lane = tid & 63;
    const int wv = __builtin_amdgcn_readfirstlane(tid >> 6);
    const int q = lane >> 4, n = lane & 15;
    const int r = blockIdx.x & 7, c = blockIdx.x >> 3;
    K2C k;
    if (c == 32) {
        if (wv != 0) return;
        const u32x4 a = *(const u32x4*)(ws + OFF_I1X + (r * 256 + n * 16 + 4 * q) * 4);
        u32x2 w[4];
#pragma unroll
        for (int i = 0; i < 4; ++i) w[i] = *(const u32x2*)(ws + OFF_WSP + (r * 256 + n + 16 * (4 * q + i)) * 8);
        k2_consts(k, r, q, n);
        f32x4 Xr, Xi;
        k2_fwd(k, a, Xr, Xi);
        float wr[4], wi[4];
#pragma unroll
        for (int i = 0; i < 4; ++i) { wr[i] = lo16(w[i][0]); wi[i] = hi16(w[i][0]); }
        const u32x4 oa = k2_inv(k, Xr, Xi, wr, wi);
#pragma unroll
        for (int i = 0; i < 4; ++i) { wr[i] = lo16(w[i][1]); wi[i] = hi16(w[i][1]); }
        const u32x4 ob = k2_inv(k, Xr, Xi, wr, wi);
#pragma unroll
        for (int i = 0; i < 4; ++i)
            *(u32x2*)(ws + OFF_I2X + (r * 256 + n + 16 * (4 * q + i)) * 8) = u32x2{oa[i], ob[i]};
        return;
    }
    char* wsm = sm;
    char* tsm = sm + 32768;
    u32x4 ld[4];
    {
        const char* I1 = ws + OFF_I1 + (size_t)(r * 32 + c) * 16384 + wv * 16;
#pragma unroll
        for (int e = 0; e < 4; ++e) ld[e] = *(const u32x4*)(I1 + (n + 16 * (4 * q + e)) * 64);
    }
    u32x4 wl[8];
    {
        const u16* wp = wv == 0 ? w0 : wv == 1 ? w1 : wv == 2 ? w2 : w3;
        const int ch = (lane & 1) ^ ((lane >> 4) & 1);
        wp += (size_t)(r + 8 * (lane >> 1)) * 513 + 16 * c + 8 * ch;
#pragma unroll
        for (int e = 0; e < 8; ++e) wl[e] = ((const U4u*)(wp + (size_t)e * 256 * 513))->v;
    }
    k2_consts(k, r, q, n);
    f32x4 Xr[4], Xi[4];
#pragma unroll
    for (int u = 0; u < 4; ++u) k2_fwd(k, u32x4{ld[0][u], ld[1][u], ld[2][u], ld[3][u]}, Xr[u], Xi[u]);
#pragma unroll
    for (int e = 0; e < 8; ++e) *(u32x4*)(wsm + wv * 8192 + e * 1024 + lane * 16) = wl[e];
    __syncthreads();
    u32x4 o[2][4];
    const char* wb = wsm + (n + 64 * q) * 32 + (((wv >> 1) ^ (n >> 3)) << 4) + (wv & 1) * 8;
#pragma unroll
    for (int f = 0; f < 2; ++f) {
        u32x2 pr[4], pi[4];
#pragma unroll
        for (int i = 0; i < 4; ++i) {
            pr[i] = *(const u32x2*)(wb + (2 * f) * 8192 + i * 512);
            pi[i] = *(const u32x2*)(wb + (2 * f + 1) * 8192 + i * 512);
        }
#pragma unroll
        for (int u = 0; u < 4; ++u) {
            float wr[4], wi[4];
#pragma unroll
            for (int i = 0; i < 4; ++i) {
                wr[i] = (u & 1) ? hi16(pr[i][u >> 1]) : lo16(pr[i][u >> 1]);
                wi[i] = (u & 1) ? hi16(pi[i][u >> 1]) : lo16(pi[i][u >> 1]);
            }
            o[f][u] = k2_inv(k, Xr[u], Xi[u], wr, wi);
        }
    }
#pragma unroll
    for (int i = 0; i < 4; ++i)
#pragma unroll
        for (int cc = 0; cc < 2; ++cc)
            *(u32x4*)(tsm + (n + 16 * (4 * q + i)) * 128 + (((2 * wv + cc) ^ (n & 7)) << 4)) =
                u32x4{o[0][2 * cc][i], o[1][2 * cc][i], o[0][2 * cc + 1][i], o[1][2 * cc + 1][i]};
    __syncthreads();
    {
        char* I2 = ws + OFF_I2 + (size_t)(64 * wv + (lane >> 3)) * I2_ROW + r * 4096 + c * 128 + (((lane & 7) ^ (lane >> 3)) << 4);
#pragma unroll
        for (int e = 0; e < 8; ++e)
            *(u32x4*)(I2 + (size_t)e * 8 * I2_ROW) = *(const u32x4*)(tsm + wv * 8192 + e * 1024 + lane * 16);
    }
}

__global__ __launch_bounds__(512) void k3(const char* __restrict__ ws, u16* __restrict__ out) {
    __shared__ __attribute__((aligned(16))) char zs[4 * ZP];
    const int tid = threadIdx.x, lane = tid & 63;
    const int wv = __builtin_amdgcn_readfirstlane(tid >> 6);
    const int q = lane >> 4, n = lane & 15;
    const int s1 = blockIdx.x;
    const int h = wv & 1, m = wv >> 1;
    const int rho = 16 * h + n;
    const f32x4 z4 = {0.f, 0.f, 0.f, 0.f};

    u32x4 ld[4];
    {
        const char* I2 = ws + OFF_I2 + (size_t)s1 * I2_ROW + (2 * q) * 4096 + (rho + 128 * m) * 8;
#pragma unroll
        for (int e = 0; e < 4; ++e) {
            const u32x2 a = *(const u32x2*)(I2 + e * 256), b = *(const u32x2*)(I2 + 4096 + e * 256);
            ld[e] = u32x4{a[0], a[1], b[0], b[1]};
        }
    }
    u32x4 ldx = {0u, 0u, 0u, 0u};
    if (wv == 7) {
        const char* I2x = ws + OFF_I2X + ((2 * q) * 256 + s1) * 8;
        const u32x2 a = *(const u32x2*)(I2x), b = *(const u32x2*)(I2x + 2048);
        ldx = u32x4{a[0], a[1], b[0], b[1]};
    }
    const f32x4 G = *(const f32x4*)(ws + OFF_G + (s1 * 4 + (n >> 2)) * 16);

    u32 fr[4], fi[4];
#pragma unroll
    for (int i = 0; i < 4; ++i) {
        const float a = (float)((4 * q + i) * n) * (1.0f / 32.0f);
        const float c = cosr(a), s = sinr(a);
        fr[i] = pk(c, -s); fi[i] = pk(s, c);
    }
    const u32 mn = (n & 1) ? 0x80008000u : 0u;
    float tc[2][4], ts[2][4];
#pragma unroll
    for (int T = 0; T < 2; ++T)
#pragma unroll
        for (int i = 0; i < 4; ++i) {
            const float a = (float)((16 * T + 4 * q + i) * rho) * (1.0f / 1024.0f);
            tc[T][i] = cosr(a); ts[T][i] = sinr(a);
        }
    u32x4 Mix;
    {
        const int pp = n >> 2, i = n & 3;
        const float sc = 1.0f / 4194304.0f;
#pragma unroll
        for (int rr = 0; rr < 2; ++rr) {
            const float a = (float)(pp * (2 * q + rr)) * 0.125f;
            const float c = cosr(a) * sc, s = sinr(a) * sc;
            const float tau = ((rr == 0) == (i < 2)) ? 1.f : -1.f;
#pragma unroll
            for (int f = 0; f < 2; ++f) {
                const float g0 = G[2 * f], g1 = tau * G[2 * f + 1];
                const float cr = c * g0 - s * g1, ci = s * g0 + c * g1;
                Mix[2 * rr + f] = (i & 1) ? (i == 1 ? pk(ci, cr) : pk(-ci, -cr)) : pk(cr, -ci);
            }
        }
    }
    {
        char* zq = zs + q * ZP;
        u32x4 dv, mv;
#pragma unroll
        for (int e = 0; e < 4; ++e) {
            f32x4 d = mma(Mix, ld[e], z4);
            if (e == 0 && wv == 0 && n == 0) { d[0] = 0.5f * (d[0] + d[2]); d[1] = 0.5f * (d[1] + d[3]); }
            dv[e] = pk(d[0], d[1]);
            mv[3 - e] = pk(d[2], d[3]);
        }
        const int rm = 32 - rho;
        *(u32x4*)(zq + rho * 128 + ((m ^ (rho & 7)) << 4)) = dv;
        *(u32x4*)(zq + rm * 128 + (((7 - m) ^ (rm & 7)) << 4)) = mv;
        if (rho == 0) {
#pragma unroll
            for (int e = 0; e < 4; ++e) {
                const int kk = 32 - 4 * m - e;
                if (kk < 32) *(u32*)(zq + kk * 4) = mv[3 - e];
            }
        }
        if (wv == 7) {
            const f32x4 d = mma(Mix, ldx, z4);
            if (n == 0) *(u32*)(zq + 64) = pk(0.5f * (d[0] + d[2]), 0.5f * (d[1] + d[3]));
        }
    }
    __syncthreads();
    const int p = m;
    const char* zp = zs + p * ZP;
    u32x4 Ad[2];
#pragma unroll
    for (int T = 0; T < 2; ++T) {
        f32x4 er = z4, ei = z4;
#pragma unroll
        for (int s = 0; s < 2; ++s) {
            const u32x4 a = *(const u32x4*)(zp + (16 * T + n) * 128 + (((4 * s + q) ^ (n & 7)) << 4));
            const u32 m0 = s ? mn : 0u, m1 = m0 ^ (h ? 0x80008000u : 0u);
            er = mma(a, u32x4{fr[0] ^ m0, fr[1] ^ m1, fr[2] ^ m0, fr[3] ^ m1}, er);
            ei = mma(a, u32x4{fi[0] ^ m0, fi[1] ^ m1, fi[2] ^ m0, fi[3] ^ m1}, ei);
        }
#pragma unroll
        for (int i = 0; i < 4; ++i)
            Ad[T][i] = pk(er[i] * tc[T][i] - ei[i] * ts[T][i], ei[i] * tc[T][i] + er[i] * ts[T][i]);
    }
    u16* o0 = out + (size_t)(s1 + 256 * p) * 1024 + 16 * h + 4 * q + 32 * n;
#pragma unroll
    for (int Tp = 0; Tp < 2; ++Tp) {
        f32x4 orr = z4, oi = z4;
#pragma unroll
        for (int T = 0; T < 2; ++T) {
            const u32 m0 = T ? mn : 0u, m1 = m0 ^ (Tp ? 0x80008000u : 0u);
            orr = mma(Ad[T], u32x4{fr[0] ^ m0, fr[1] ^ m1, fr[2] ^ m0, fr[3] ^ m1}, orr);
            oi = mma(Ad[T], u32x4{fi[0] ^ m0, fi[1] ^ m1, fi[2] ^ m0, fi[3] ^ m1}, oi);
        }
        *(u32x2*)(o0 + 512 * Tp) = u32x2{pk(orr[0], orr[1]), pk(orr[2], orr[3])};
        *(u32x2*)(o0 + 1024 * 1024 + 512 * Tp) = u32x2{pk(oi[0], oi[1]), pk(oi[2], oi[3])};
    }
}

extern "C" void kernel_launch(void* const* d_in, const int* in_sizes, int n_in,
                              void* d_out, int out_size, void* d_ws, size_t ws_size,
                              hipStream_t stream) {
    const u16* x = (const u16*)d_in[0];
    const u16* war = (const u16*)d_in[1];
    const u16* wai = (const u16*)d_in[2];
    const u16* wbr = (const u16*)d_in[4];
    const u16* wbi = (const u16*)d_in[5];
    const u16* gw = (const u16*)d_in[6];
    char* ws = (char*)d_ws;
    k1<<<256, 512, 0, stream>>>(x, gw, war, wai, wbr, wbi, ws);
    k2<<<264, 256, 0, stream>>>(war, wai, wbr, wbi, ws);
    k3<<<256, 512, 0, stream>>>(ws, (u16*)d_out);
}
```

```cpp
#include <hip/hip_runtime.h>
#include <hip/hip_bf16.h>
#include <stdint.h>

typedef unsigned short u16;
typedef unsigned int u32;
typedef __attribute__((ext_vector_type(8))) __bf16 bf16x8;
typedef __attribute__((ext_vector_type(2))) __bf16 bf16x2;
typedef __attribute__((ext_vector_type(4))) short s16x4;
typedef __attribute__((ext_vector_type(4))) float f32x4;
typedef __attribute__((ext_vector_type(2))) float f32x2;
typedef __attribute__((ext_vector_type(4))) unsigned u32x4;
typedef __attribute__((ext_vector_type(2))) unsigned u32x2;
#define LDSP __attribute__((address_space(3)))
#define DI __device__ __forceinline__

DI u32 pk(float lo, float hi) {
    f32x2 t = {lo, hi};
    return __builtin_bit_cast(u32, __builtin_convertvector(t, bf16x2));
}
DI float bfr(float v) { return (float)(__bf16)v; }
DI f32x4 mma(u32x4 a, u32x4 b, f32x4 c) {
    return __builtin_amdgcn_mfma_f32_16x16x32_bf16(__builtin_bit_cast(bf16x8, a), __builtin_bit_cast(bf16x8, b), c, 0, 0, 0);
}
DI f32x4 mma16(u32x2 a, u32x2 b, f32x4 c) {
    return __builtin_amdgcn_mfma_f32_16x16x16bf16_1k(__builtin_bit_cast(s16x4, a), __builtin_bit_cast(s16x4, b), c, 0, 0, 0);
}
DI float dot2(u32 a, u32 b, float c) {
    return __builtin_amdgcn_fdot2_f32_bf16(__builtin_bit_cast(bf16x2, a), __builtin_bit_cast(bf16x2, b), c, false);
}
DI float cosr(float x) { return __builtin_amdgcn_cosf(x); }
DI float sinr(float x) { return __builtin_amdgcn_sinf(x); }
DI float lo16(u32 v) { return __builtin_bit_cast(float, v << 16); }
DI float hi16(u32 v) { return __builtin_bit_cast(float, v & 0xffff0000u); }
DI u32x2 trd(const LDSP char* p) {
    return __builtin_bit_cast(u32x2, __builtin_amdgcn_ds_read_tr16_b64_v4i16((LDSP s16x4*)p));
}
#define BAR() asm volatile("s_waitcnt lgkmcnt(0)\n\ts_barrier" ::: "memory")
#define PIN() __builtin_amdgcn_sched_barrier(0)
#define KEEP(x) asm volatile("" : "+v"(x))

static constexpr u32 TAG = 0xFFAB7F95u;
static constexpr int SC1 = 16;
static constexpr int ROW = 32768 + 128;
static constexpr int WS_BYTES = 256 * ROW;
static constexpr int ZP = 33 * 128;
static constexpr int L_XS = 0;
static constexpr int L_ZS = 32768;
static constexpr int L_WT = L_ZS + 4 * ZP;
static constexpr int L_T1 = L_WT + 4 * 8320;
static constexpr int L_T2 = L_T1 + 4 * 4640;
static constexpr int L_G = L_T2 + 32768;
static constexpr int L_END = L_G + 64;
static constexpr unsigned SPIN_CAP = 1u << 16;
static constexpr u32 NEG2 = 0x80008000u;

struct K2C {
    u32x4 F1r, F1i, F2r, F2i, G1r, G1i, G2r, G2i;
    f32x4 t1c, t1s, t2c, t2s;
};

DI u32x4 k2_inv(const K2C& k, f32x4 Xr, f32x4 Xi, f32x4 wr, f32x4 wi) {
    const f32x4 z4 = {0.f, 0.f, 0.f, 0.f};
    const f32x4 yr = Xr * wr - Xi * wi, yi = Xr * wi + Xi * wr;
    const u32x4 y = {pk(yr[0], yi[0]), pk(yr[1], yi[1]), pk(yr[2], yi[2]), pk(yr[3], yi[3])};
    const f32x4 er = mma(y, k.G1r, z4), ei = mma(y, k.G1i, z4);
    const f32x4 br = er * k.t2c - ei * k.t2s, bi = ei * k.t2c + er * k.t2s;
    const u32x4 b = {pk(br[0], bi[0]), pk(br[1], bi[1]), pk(br[2], bi[2]), pk(br[3], bi[3])};
    const f32x4 orr = mma(k.G2r, b, z4), oi = mma(k.G2i, b, z4);
    return u32x4{pk(orr[0], oi[0]), pk(orr[1], oi[1]), pk(orr[2], oi[2]), pk(orr[3], oi[3])};
}

__global__ __launch_bounds__(512) void gf_fused(const u16* __restrict__ x, const u16* __restrict__ gw,
                                                const u16* __restrict__ w0, const u16* __restrict__ w1,
                                                const u16* __restrict__ w2, const u16* __restrict__ w3,
                                                char* ws, u16* out) {
    __shared__ __attribute__((aligned(16))) char smem[L_END];
    LDSP char* sm = (LDSP char*)smem;
    const int tid = threadIdx.x, lane = tid & 63;
    const int wv = __builtin_amdgcn_readfirstlane(tid >> 6);
    const int p = wv >> 1, h = wv & 1;
    const int q = lane >> 4, n = lane & 15;
    const int id = blockIdx.x;
    const int s1 = (id & 7) * 32 + (id >> 3);
    const int r = id & 7, c = id >> 3;
    const bool xh = (c == 31) && (wv == 7);
    const f32x4 z4 = {0.f, 0.f, 0.f, 0.f};
    const __amdgpu_buffer_rsrc_t rs = __builtin_amdgcn_make_buffer_rsrc(ws, 0, WS_BYTES, 0x00020000);

    const int gch = lane ^ (((lane >> 4) & 1) << 1);
    {
        const u16* r0 = x + (size_t)(s1 + 256 * p) * 1024;
        const u16* r1 = r0 + (size_t)1024 * 1024;
        const u32 vo = gch * 16;
        const u32 l0 = (u32)(size_t)(sm + L_XS) + wv * 4096;
        u32 keep;
        asm volatile("s_mov_b32 %0, m0\n\ts_mov_b32 m0, %4\n\ts_nop 0\n\t"
                     "global_load_lds_dwordx4 %1, %2\n\tglobal_load_lds_dwordx4 %1, %2 offset:1024\n\t"
                     "s_add_u32 m0, m0, 2048\n\ts_nop 0\n\t"
                     "global_load_lds_dwordx4 %1, %3\n\tglobal_load_lds_dwordx4 %1, %3 offset:1024\n\t"
                     "s_mov_b32 m0, %0"
                     : "=&s"(keep) : "v"(vo), "s"(r0), "s"(r1), "s"(l0) : "memory", "scc");
    }
    PIN();
    const __amdgpu_buffer_rsrc_t rg = __builtin_amdgcn_make_buffer_rsrc((void*)gw, 0, 2048, 0x00020000);
    const u32x4 gw0 = __builtin_amdgcn_raw_buffer_load_b128(rg, gch * 16, 0, 0);
    const u32x4 gw1 = __builtin_amdgcn_raw_buffer_load_b128(rg, gch * 16 + 1024, 0, 0);
    u32x4 wl[4];
    {
        const u16* wp = p == 0 ? w0 : p == 1 ? w1 : p == 2 ? w2 : w3;
        const __amdgpu_buffer_rsrc_t rw = __builtin_amdgcn_make_buffer_rsrc((void*)wp, 0, 2048 * 513 * 2, 0x00020000);
        const int tp = (lane >> 3) + 16 * ((lane >> 1) & 3) + 128 * h;
        const int vo = ((r + 8 * tp) * 513 + 16 * c + 8 * (lane & 1)) * 2;
#pragma unroll
        for (int e = 0; e < 4; ++e)
            wl[e] = __builtin_amdgcn_raw_buffer_load_b128(rw, vo, (8 * (e & 1) + 64 * (e >> 1)) * 8 * 513 * 2, 0);
    }
    PIN();

    const int rho = 16 * h + n;
    const u32 mn = (n & 1) ? NEG2 : 0u;
    f32x4 tc[2], ts[2];
    float c4[4], s4[4];
#pragma unroll
    for (int i = 0; i < 4; ++i) {
        const float a = (float)((4 * q + i) * n) * (1.0f / 32.0f);
        c4[i] = cosr(a); s4[i] = sinr(a);
    }
#pragma unroll
    for (int T = 0; T < 2; ++T)
#pragma unroll
        for (int i = 0; i < 4; ++i) {
            const float a = (float)((16 * T + 4 * q + i) * rho) * (1.0f / 1024.0f);
            tc[T][i] = cosr(a); ts[T][i] = sinr(a);
        }
    {
        const u32 c01 = pk(c4[0], c4[1]), c23 = pk(c4[2], c4[3]), s01 = pk(s4[0], s4[1]), s23 = pk(s4[2], s4[3]);
        const u32 mh = h ? 0x80000000u : 0u;
        u32x4 Cf = {c01 ^ mh, c23 ^ mh, c01 ^ mh ^ mn, c23 ^ mh ^ mn};
        u32x4 Sf = {s01 ^ mh, s23 ^ mh, s01 ^ mh ^ mn, s23 ^ mh ^ mn};
        u32x4 Nf = Sf ^ NEG2;
        u32x4 Are[2][2], Aim[2][2];
#pragma unroll
        for (int Tp = 0; Tp < 2; ++Tp)
#pragma unroll
            for (int T = 0; T < 2; ++T) {
                const u32 m = (Tp ? 0x80000000u : 0u) ^ (T ? mn : 0u);
                Are[Tp][T] = u32x4{c01 ^ m, c23 ^ m, s01 ^ m, s23 ^ m};
                Aim[Tp][T] = u32x4{s01 ^ m ^ NEG2, s23 ^ m ^ NEG2, c01 ^ m, c23 ^ m};
                KEEP(Are[Tp][T]); KEEP(Aim[Tp][T]);
            }
        u32x2 Sep;
        {
            const int rr = n >> 1, part = n & 1, sg = (rr & 1) ? -1 : 1;
            const float aa = (float)(q * rr + sg) * 0.125f, ab = (float)(q * rr - sg) * 0.125f;
            const float R2 = 1.41421356237f;
            const float ar = R2 * cosr(aa), ai = -R2 * sinr(aa), br = R2 * cosr(ab), bi = -R2 * sinr(ab);
            Sep[0] = part ? pk(ai, ar) : pk(ar, -ai);
            Sep[1] = part ? pk(bi, -br) : pk(br, bi);
        }
        KEEP(Cf); KEEP(Sf); KEEP(Nf); KEEP(Sep); KEEP(tc[0]); KEEP(tc[1]); KEEP(ts[0]); KEEP(ts[1]);
        const LDSP char* xs = sm + L_XS + wv * 4096;
        const LDSP char* xb0 = xs + 64 * (4 * q + (n >> 2)) + 32 * (q & 1) + 8 * (n & 3);
        const LDSP char* xb1 = xs + 64 * (4 * q + (n >> 2)) + 32 * ((q & 1) ^ 1) + 8 * (n & 3);
        LDSP char* zw = sm + L_ZS + p * ZP + rho * 128;
        PIN();
        asm volatile("s_waitcnt vmcnt(6)" ::: "memory");
        PIN();
        u32x4 Bd[2];
#pragma unroll
        for (int T = 0; T < 2; ++T) {
            const LDSP char* b = T ? xb1 : xb0;
            const u32x2 a0 = trd(b), a1 = trd(b + 1024), a2 = trd(b + 2048), a3 = trd(b + 3072);
            const u32x4 xr = {a0[0], a0[1], a1[0], a1[1]}, xi = {a2[0], a2[1], a3[0], a3[1]};
            f32x4 Dr = mma(xr, Cf, z4); Dr = mma(xi, Sf, Dr);
            f32x4 Di = mma(xr, Nf, z4); Di = mma(xi, Cf, Di);
            const f32x4 re = Dr * tc[T] + Di * ts[T], im = Di * tc[T] - Dr * ts[T];
            Bd[T] = u32x4{pk(re[0], re[1]), pk(re[2], re[3]), pk(im[0], im[1]), pk(im[2], im[3])};
        }
#pragma unroll
        for (int Tp = 0; Tp < 2; ++Tp) {
            f32x4 Zr = mma(Are[Tp][0], Bd[0], z4); Zr = mma(Are[Tp][1], Bd[1], Zr);
            f32x4 Zi = mma(Aim[Tp][0], Bd[0], z4); Zi = mma(Aim[Tp][1], Bd[1], Zi);
            const u32x4 o = {pk(Zr[0], Zi[0]), pk(Zr[1], Zi[1]), pk(Zr[2], Zi[2]), pk(Zr[3], Zi[3])};
            *(LDSP u32x4*)(zw + (((4 * Tp + q) ^ (rho & 7)) << 4)) = o;
            if (rho == 0) {
                if (Tp == 1) {
#pragma unroll
                    for (int i = 0; i < 4; ++i) *(LDSP u32*)(zw + 32 * 128 + ((15 + 4 * q + i) << 2)) = o[i];
                } else if (q == 0) {
                    *(LDSP u32*)(zw + 32 * 128 + 31 * 4) = o[0];
                }
            }
        }
        BAR();
        {
            const int rm = 32 - rho;
            const LDSP char* zq = sm + L_ZS + q * ZP;
            const u32x4 dv = *(const LDSP u32x4*)(zq + rho * 128 + ((p ^ (rho & 7)) << 4));
            const u32x4 mv = *(const LDSP u32x4*)(zq + rm * 128 + (((7 - p) ^ (rm & 7)) << 4));
            const int vo = s1 * ROW + (n >> 1) * 4096 + (h + 8 * p) * 128 + q * 32 + (n & 1) * 16;
#pragma unroll
            for (int e = 0; e < 4; ++e) {
                const f32x4 d = mma16(u32x2{dv[e], mv[3 - e]}, Sep, z4);
                __builtin_amdgcn_raw_buffer_store_b128(u32x4{pk(d[0], d[1]), TAG, pk(d[2], d[3]), TAG}, rs, vo + e * 256, 0, SC1);
            }
            if (wv == 7) {
                const u32 v = *(const LDSP u32*)(zq + 64);
                const f32x4 d = mma16(u32x2{v, v}, Sep, z4);
                if (q == 0) __builtin_amdgcn_raw_buffer_store_b64(u32x2{pk(d[0], d[0]), TAG}, rs, s1 * ROW + 32768 + n * 8, 0, SC1);
            }
        }
    }
    {
        const LDSP char* xs = sm + L_XS + wv * 4096 + 2048 * h + lane * 16;
        const u32x4 x0 = *(const LDSP u32x4*)(xs), x1 = *(const LDSP u32x4*)(xs + 1024);
        float acc = 0.f;
#pragma unroll
        for (int i = 0; i < 4; ++i) { acc = dot2(x0[i], gw0[i], acc); acc = dot2(x1[i], gw1[i], acc); }
#pragma unroll
        for (int o = 32; o > 0; o >>= 1) acc += __shfl_xor(acc, o);
        const float z = bfr(acc);
        const float g = bfr(1.0f / (1.0f + __expf(-z)));
        const float og = bfr(1.0f - g);
        if (lane == 0) {
            LDSP float* G = (LDSP float*)(sm + L_G) + p * 4;
            G[h] = g; G[2 + h] = og;
        }
    }
    {
        LDSP char* wt = sm + L_WT + p * 8320 + (lane & 1) * 4160 + ((lane >> 3) * 4 + ((lane >> 1) & 3) + 128 * h) * 4;
#pragma unroll
        for (int e = 0; e < 4; ++e)
#pragma unroll
            for (int j = 0; j < 4; ++j)
                *(LDSP u32*)(wt + j * 1024 + (32 * (e & 1) + 64 * (e >> 1)) * 4) = wl[e][j];
    }
    u16 wx[4][4];
    if (xh) {
#pragma unroll
        for (int a = 0; a < 4; ++a) {
            const u16* wp = a == 0 ? w0 : a == 1 ? w1 : a == 2 ? w2 : w3;
#pragma unroll
            for (int i = 0; i < 4; ++i) wx[a][i] = wp[(size_t)(r + 8 * (n + 16 * i + 64 * q)) * 513 + 512];
        }
    }
    K2C k;
    {
        float c1[4], s1f[4];
#pragma unroll
        for (int i = 0; i < 4; ++i) {
            const int e = 4 * q + i, a = 8 * (i >> 1) + 2 * q + (i & 1);
            const float ru = (float)(r + 8 * n);
            c1[i] = cosr((float)e * ru * (1.0f / 128.0f)); s1f[i] = sinr((float)e * ru * (1.0f / 128.0f));
            k.t1c[i] = cosr((float)a * ru * (1.0f / 2048.0f)); k.t1s[i] = sinr((float)a * ru * (1.0f / 2048.0f));
            float cc = cosr((float)(a * n) * (1.0f / 16.0f)), ss = sinr((float)(a * n) * (1.0f / 16.0f));
            k.F2r[i] = pk(cc, ss); k.F2i[i] = pk(-ss, cc);
            cc = cosr((float)(e * n) * (1.0f / 16.0f)); ss = sinr((float)(e * n) * (1.0f / 16.0f));
            k.G1r[i] = pk(cc, -ss); k.G1i[i] = pk(ss, cc);
            const float a3 = (float)(n * (r + 8 * e));
            k.t2c[i] = cosr(a3 * (1.0f / 2048.0f)); k.t2s[i] = sinr(a3 * (1.0f / 2048.0f));
            cc = cosr(a3 * (1.0f / 128.0f)); ss = sinr(a3 * (1.0f / 128.0f));
            k.G2r[i] = pk(cc, -ss); k.G2i[i] = pk(ss, cc);
        }
        k.F1r = u32x4{pk(c1[0], c1[1]), pk(c1[2], c1[3]), pk(s1f[0], s1f[1]), pk(s1f[2], s1f[3])};
        k.F1i = u32x4{k.F1r[2] ^ NEG2, k.F1r[3] ^ NEG2, k.F1r[0], k.F1r[1]};
        KEEP(k.F1r); KEEP(k.F1i); KEEP(k.F2r); KEEP(k.F2i); KEEP(k.G1r); KEEP(k.G1i); KEEP(k.G2r); KEEP(k.G2i);
        KEEP(k.t1c); KEEP(k.t1s); KEEP(k.t2c); KEEP(k.t2s);
    }
    BAR();
    f32x4 W[4][2];
    {
        const LDSP char* wt = sm + L_WT + (wv >> 2) * 4160 + (wv & 3) * 1024 + lane * 16;
#pragma unroll
        for (int a = 0; a < 4; ++a) {
            const u32x4 v = *(const LDSP u32x4*)(wt + a * 8320);
            W[a][0] = f32x4{lo16(v[0]), lo16(v[1]), lo16(v[2]), lo16(v[3])};
            W[a][1] = f32x4{hi16(v[0]), hi16(v[1]), hi16(v[2]), hi16(v[3])};
            KEEP(W[a][0]); KEEP(W[a][1]);
        }
    }
    const int lo2 = (32 * wv + (lane >> 3)) * ROW + (r * 32 + c) * 128;
    PIN();
    asm volatile("s_waitcnt vmcnt(0)" ::: "memory");
    PIN();
    {
        u32x4 a[4];
        for (unsigned spins = 0;;) {
#pragma unroll
            for (int e = 0; e < 4; ++e) a[e] = __builtin_amdgcn_raw_buffer_load_b128(rs, lo2 + (lane & 7) * 16, e * 8 * ROW, SC1);
            bool ok = true;
#pragma unroll
            for (int e = 0; e < 4; ++e) ok = ok && a[e][1] == TAG && a[e][3] == TAG;
            if (__all(ok) || ++spins > SPIN_CAP) break;
            __builtin_amdgcn_s_sleep(2);
        }
        LDSP char* t1 = sm + L_T1 + ((lane >> 1) & 3) * 4640 + (lane & 1) * 1040 + wv * 64 + (lane >> 3) * 4;
#pragma unroll
        for (int e = 0; e < 4; ++e) {
            *(LDSP u32*)(t1 + (e & 1) * 512 + (e >> 1) * 32) = a[e][0];
            *(LDSP u32*)(t1 + 2304 + (e & 1) * 512 + (e >> 1) * 32) = a[e][2];
        }
    }
    BAR();
    {
        const LDSP char* t1 = sm + L_T1 + p * 4640 + h * 2304 + lane * 8;
        f32x4 dr[2], di[2];
#pragma unroll
        for (int t = 0; t < 2; ++t) {
            const u32x2 a0 = trd(t1 + t * 512), a1 = trd(t1 + 1040 + t * 512);
            const u32x4 a = {a0[0], a0[1], a1[0], a1[1]};
            const f32x4 er = mma(a, k.F1r, z4), ei = mma(a, k.F1i, z4);
            const f32x4 c4v = {k.t1c[2 * t], k.t1c[2 * t], k.t1c[2 * t + 1], k.t1c[2 * t + 1]};
            const f32x4 s4v = {k.t1s[2 * t], k.t1s[2 * t], k.t1s[2 * t + 1], k.t1s[2 * t + 1]};
            dr[t] = er * c4v + ei * s4v; di[t] = ei * c4v - er * s4v;
        }
        u32x4 o[2][2];
#pragma unroll
        for (int u = 0; u < 2; ++u) {
            const u32x4 b = {pk(dr[0][u], di[0][u]), pk(dr[0][2 + u], di[0][2 + u]), pk(dr[1][u], di[1][u]), pk(dr[1][2 + u], di[1][2 + u])};
            const f32x4 Xr = mma(k.F2r, b, z4), Xi = mma(k.F2i, b, z4);
            o[0][u] = k2_inv(k, Xr, Xi, W[0][u], W[1][u]);
            o[1][u] = k2_inv(k, Xr, Xi, W[2][u], W[3][u]);
        }
        LDSP char* t2 = sm + L_T2 + (n + 64 * q) * 128 + ((wv ^ (n & 7)) << 4);
#pragma unroll
        for (int i = 0; i < 4; ++i)
            *(LDSP u32x4*)(t2 + i * 2048) = u32x4{o[0][0][i], o[1][0][i], o[0][1][i], o[1][1][i]};
        BAR();
        const LDSP char* t2r = sm + L_T2 + wv * 4096 + lane * 16;
        const int vo = lo2 + (((lane & 7) ^ (lane >> 3)) << 4);
#pragma unroll
        for (int e = 0; e < 4; ++e)
            __builtin_amdgcn_raw_buffer_store_b128(*(const LDSP u32x4*)(t2r + e * 1024), rs, vo, e * 8 * ROW, SC1);
    }
    if (xh) {
        const int a_ = 8 * ((n >> 1) & 1) + 2 * (n >> 2) + (n & 1);
        const int vo = (a_ + 64 * q) * ROW + 32768 + r * 16;
        u32x4 a[4];
        for (unsigned spins = 0;;) {
#pragma unroll
            for (int e = 0; e < 4; ++e) a[e] = __builtin_amdgcn_raw_buffer_load_b128(rs, vo, e * 16 * ROW, SC1);
            bool ok = true;
#pragma unroll
            for (int e = 0; e < 4; ++e) ok = ok && a[e][1] == TAG && a[e][3] == TAG;
            if (__all(ok) || ++spins > SPIN_CAP) break;
            __builtin_amdgcn_s_sleep(2);
        }
        const u32x4 f = {(a[0][0] & 0xffffu) | (a[1][0] & 0xffff0000u), (a[2][0] & 0xffffu) | (a[3][0] & 0xffff0000u),
                         (a[0][2] & 0xffffu) | (a[1][2] & 0xffff0000u), (a[2][2] & 0xffffu) | (a[3][2] & 0xffff0000u)};
        const f32x4 er = mma(f, k.F1r, z4), ei = mma(f, k.F1i, z4);
        const f32x4 dr = er * k.t1c + ei * k.t1s, di = ei * k.t1c - er * k.t1s;
        const u32x4 b = {pk(dr[0], di[0]), pk(dr[1], di[1]), pk(dr[2], di[2]), pk(dr[3], di[3])};
        const f32x4 Xr = mma(k.F2r, b, z4), Xi = mma(k.F2i, b, z4);
        f32x4 wf[4];
#pragma unroll
        for (int a2 = 0; a2 < 4; ++a2) wf[a2] = f32x4{lo16(wx[a2][0]), lo16(wx[a2][1]), lo16(wx[a2][2]), lo16(wx[a2][3])};
        const u32x4 oa = k2_inv(k, Xr, Xi, wf[0], wf[1]), ob = k2_inv(k, Xr, Xi, wf[2], wf[3]);
        const int vs = (n + 64 * q) * ROW + 32768 + r * 16;
#pragma unroll
        for (int i = 0; i < 4; ++i)
            __builtin_amdgcn_raw_buffer_store_b128(u32x4{oa[i], ob[i], 0u, 0u}, rs, vs, i * 16 * ROW, SC1);
    }
    u32x4 Fr[2][2], Fi[2][2], Er_[2], Ei_[2], Mix;
    {
        u32 fr[4], fi[4];
#pragma unroll
        for (int i = 0; i < 4; ++i) { fr[i] = pk(c4[i], -s4[i]); fi[i] = pk(s4[i], c4[i]); }
#pragma unroll
        for (int Tp = 0; Tp < 2; ++Tp)
#pragma unroll
            for (int T = 0; T < 2; ++T) {
                const u32 m0 = T ? mn : 0u, m1 = m0 ^ (Tp ? NEG2 : 0u);
                Fr[Tp][T] = u32x4{fr[0] ^ m0, fr[1] ^ m1, fr[2] ^ m0, fr[3] ^ m1};
                Fi[Tp][T] = u32x4{fi[0] ^ m0, fi[1] ^ m1, fi[2] ^ m0, fi[3] ^ m1};
                KEEP(Fr[Tp][T]); KEEP(Fi[Tp][T]);
            }
#pragma unroll
        for (int s = 0; s < 2; ++s) {
            const u32 m0 = s ? mn : 0u, m1 = m0 ^ (h ? NEG2 : 0u);
            Er_[s] = u32x4{fr[0] ^ m0, fr[1] ^ m1, fr[2] ^ m0, fr[3] ^ m1};
            Ei_[s] = u32x4{fi[0] ^ m0, fi[1] ^ m1, fi[2] ^ m0, fi[3] ^ m1};
            KEEP(Er_[s]); KEEP(Ei_[s]);
        }
        const f32x4 G = *(const LDSP f32x4*)(sm + L_G + (n >> 2) * 16);
        const int pp = n >> 2, i = n & 3;
        const float sc = 1.0f / 4194304.0f;
#pragma unroll
        for (int rr = 0; rr < 2; ++rr) {
            const float a = (float)(pp * (2 * q + rr)) * 0.125f;
            const float cc = cosr(a) * sc, ss = sinr(a) * sc;
            const float tau = ((rr == 0) == (i < 2)) ? 1.f : -1.f;
#pragma unroll
            for (int f = 0; f < 2; ++f) {
                const float g0 = G[2 * f], g1 = tau * G[2 * f + 1];
                const float cr = cc * g0 - ss * g1, ci = ss * g0 + cc * g1;
                Mix[2 * rr + f] = (i & 1) ? (i == 1 ? pk(ci, cr) : pk(-ci, -cr)) : pk(cr, -ci);
            }
        }
        KEEP(Mix);
    }
    PIN();
    asm volatile("s_waitcnt vmcnt(0)" ::: "memory");
    PIN();
    {
        const int vo = s1 * ROW + (2 * q) * 4096 + (rho + 128 * p) * 8;
        u32x2 a[8];
        for (unsigned spins = 0;;) {
#pragma unroll
            for (int e = 0; e < 8; ++e) a[e] = __builtin_amdgcn_raw_buffer_load_b64(rs, vo + (e >> 1) * 256, (e & 1) * 4096, SC1);
            bool ok = true;
#pragma unroll
            for (int e = 0; e < 8; ++e) ok = ok && a[e][1] != TAG;
            if (__all(ok) || ++spins > SPIN_CAP) break;
            __builtin_amdgcn_s_sleep(2);
        }
        LDSP char* zq = sm + L_ZS + q * ZP;
        u32x4 dv, mv;
#pragma unroll
        for (int e = 0; e < 4; ++e) {
            f32x4 d = mma(Mix, u32x4{a[2 * e][0], a[2 * e][1], a[2 * e + 1][0], a[2 * e + 1][1]}, z4);
            if (e == 0 && wv == 0 && n == 0) { d[0] = 0.5f * (d[0] + d[2]); d[1] = 0.5f * (d[1] + d[3]); }
            dv[e] = pk(d[0], d[1]);
            mv[3 - e] = pk(d[2], d[3]);
        }
        const int rm = 32 - rho;
        *(LDSP u32x4*)(zq + rho * 128 + ((p ^ (rho & 7)) << 4)) = dv;
        *(LDSP u32x4*)(zq + rm * 128 + (((7 - p) ^ (rm & 7)) << 4)) = mv;
        if (rho == 0) {
#pragma unroll
            for (int e = 0; e < 4; ++e) {
                const int kk = 32 - 4 * p - e;
                if (kk < 32) *(LDSP u32*)(zq + kk * 4) = mv[3 - e];
            }
        }
        if (wv == 7) {
            const int vx = s1 * ROW + 32768 + q * 32;
            u32x2 b0, b1;
            for (unsigned spins = 0;;) {
                b0 = __builtin_amdgcn_raw_buffer_load_b64(rs, vx, 0, SC1);
                b1 = __builtin_amdgcn_raw_buffer_load_b64(rs, vx + 16, 0, SC1);
                if (__all(b0[1] != TAG && b1[1] != TAG) || ++spins > SPIN_CAP) break;
                __builtin_amdgcn_s_sleep(2);
            }
            const f32x4 d = mma(Mix, u32x4{b0[0], b0[1], b1[0], b1[1]}, z4);
            if (n == 0) *(LDSP u32*)(zq + 64) = pk(0.5f * (d[0] + d[2]), 0.5f * (d[1] + d[3]));
        }
    }
    BAR();
    {
        const LDSP char* zp = sm + L_ZS + p * ZP + n * 128;
        u32x4 Ad[2];
#pragma unroll
        for (int T = 0; T < 2; ++T) {
            const u32x4 a0 = *(const LDSP u32x4*)(zp + T * 2048 + ((q ^ (n & 7)) << 4));
            const u32x4 a1 = *(const LDSP u32x4*)(zp + T * 2048 + (((4 + q) ^ (n & 7)) << 4));
            f32x4 er = mma(a0, Er_[0], z4); er = mma(a1, Er_[1], er);
            f32x4 ei = mma(a0, Ei_[0], z4); ei = mma(a1, Ei_[1], ei);
            const f32x4 br = er * tc[T] - ei * ts[T], bi = ei * tc[T] + er * ts[T];
            Ad[T] = u32x4{pk(br[0], bi[0]), pk(br[1], bi[1]), pk(br[2], bi[2]), pk(br[3], bi[3])};
        }
        u16* o0 = out + (size_t)(s1 + 256 * p) * 1024 + 16 * h + 4 * q + 32 * n;
#pragma unroll
        for (int Tp = 0; Tp < 2; ++Tp) {
            f32x4 orr = mma(Ad[0], Fr[Tp][0], z4); orr = mma(Ad[1], Fr[Tp][1], orr);
            f32x4 oi = mma(Ad[0], Fi[Tp][0], z4); oi = mma(Ad[1], Fi[Tp][1], oi);
            *(u32x2*)(o0 + 512 * Tp) = u32x2{pk(orr[0], orr[1]), pk(orr[2], orr[3])};
            *(u32x2*)(o0 + 1024 * 1024 + 512 * Tp) = u32x2{pk(oi[0], oi[1]), pk(oi[2], oi[3])};
        }
    }
}

extern "C" void kernel_launch(void* const* d_in, const int* in_sizes, int n_in,
                              void* d_out, int out_size, void* d_ws, size_t ws_size,
                              hipStream_t stream) {
    gf_fused<<<256, 512, 0, stream>>>((const u16*)d_in[0], (const u16*)d_in[6], (const u16*)d_in[1], (const u16*)d_in[2],
                                      (const u16*)d_in[4], (const u16*)d_in[5], (char*)d_ws, (u16*)d_out);
}
```

```cpp
#define WARMUP 0
#define OUTAUX 0
#include <hip/hip_runtime.h>
#include <hip/hip_bf16.h>
#include <stdint.h>

typedef unsigned short u16;
typedef unsigned int u32;
typedef unsigned long long u64;
typedef __attribute__((ext_vector_type(8))) __bf16 bf16x8;
typedef __attribute__((ext_vector_type(2))) __bf16 bf16x2;
typedef __attribute__((ext_vector_type(4))) short s16x4;
typedef __attribute__((ext_vector_type(4))) float f32x4;
typedef __attribute__((ext_vector_type(2))) float f32x2;
typedef __attribute__((ext_vector_type(4))) unsigned u32x4;
typedef __attribute__((ext_vector_type(2))) unsigned u32x2;
#define LDSP __attribute__((address_space(3)))
#define DI __device__ __forceinline__

DI u32 pk(float lo, float hi) {
    f32x2 t = {lo, hi};
    return __builtin_bit_cast(u32, __builtin_convertvector(t, bf16x2));
}
DI u32x4 pk4(f32x4 a, f32x4 b) { return u32x4{pk(a[0], b[0]), pk(a[1], b[1]), pk(a[2], b[2]), pk(a[3], b[3])}; }
DI float bfr(float v) { return (float)(__bf16)v; }
DI f32x4 mma(u32x4 a, u32x4 b, f32x4 c) {
    return __builtin_amdgcn_mfma_f32_16x16x32_bf16(__builtin_bit_cast(bf16x8, a), __builtin_bit_cast(bf16x8, b), c, 0, 0, 0);
}
DI f32x4 mma16(u32x2 a, u32x2 b, f32x4 c) {
    return __builtin_amdgcn_mfma_f32_16x16x16bf16_1k(__builtin_bit_cast(s16x4, a), __builtin_bit_cast(s16x4, b), c, 0, 0, 0);
}
DI float dot2(u32 a, u32 b, float c) {
    return __builtin_amdgcn_fdot2_f32_bf16(__builtin_bit_cast(bf16x2, a), __builtin_bit_cast(bf16x2, b), c, false);
}
DI float cosr(float x) { return __builtin_amdgcn_cosf(x); }
DI float sinr(float x) { return __builtin_amdgcn_sinf(x); }
DI float lo16(u32 v) { return __builtin_bit_cast(float, v << 16); }
DI float hi16(u32 v) { return __builtin_bit_cast(float, v & 0xffff0000u); }
DI u32x2 trd(const LDSP char* p) {
    return __builtin_bit_cast(u32x2, __builtin_amdgcn_ds_read_tr16_b64_v4i16((LDSP s16x4*)p));
}
template <int CTRL, int RMASK> DI float dpp_add(float v) {
    const int t = __builtin_amdgcn_update_dpp(0, __builtin_bit_cast(int, v), CTRL, RMASK, 0xf, false);
    return v + __builtin_bit_cast(float, t);
}
#define BAR() asm volatile("s_waitcnt lgkmcnt(0)\n\ts_barrier" ::: "memory")
#define PIN() __builtin_amdgcn_sched_barrier(0)
#define KEEP(x) asm volatile("" : "+v"(x))

static constexpr u32 TAG = 0xFFAB7F95u;
static constexpr int SC1 = 16;
static constexpr int ROW = 32768 + 384;
static constexpr int WS_BYTES = 256 * ROW;
static constexpr int ZP = 33 * 128;
static constexpr int L_XS = 0;
static constexpr int L_ZS = 32768;
static constexpr int L_WT = L_ZS + 4 * ZP;
static constexpr int L_T1 = L_WT + 4 * 8320;
static constexpr int L_T2 = L_T1 + 4 * 4224;
static constexpr int L_KC = L_T2 + 32768;
static constexpr int L_G = L_KC + 12288;
static constexpr int L_DUM = L_G + 64;
static constexpr int L_END = L_DUM + 256;
static constexpr unsigned SPIN_CAP = 1u << 16;
static constexpr u32 NEG2 = 0x80008000u;

struct K2C {
    u32x4 F1r, F1i, F2r, F2i, G1r, G1i, G2r, G2i;
    f32x4 t1c, t1s, t2c, t2s;
};

DI u32x4 k2_inv(const K2C& k, f32x4 Xr, f32x4 Xi, f32x4 wr, f32x4 wi) {
    const f32x4 z4 = {0.f, 0.f, 0.f, 0.f};
    const u32x4 y = pk4(Xr * wr - Xi * wi, Xr * wi + Xi * wr);
    const f32x4 er = mma(y, k.G1r, z4), ei = mma(y, k.G1i, z4);
    const u32x4 b = pk4(er * k.t2c - ei * k.t2s, ei * k.t2c + er * k.t2s);
    return pk4(mma(k.G2r, b, z4), mma(k.G2i, b, z4));
}

__global__ __launch_bounds__(512) void gf_fused(const u16* __restrict__ x, const u16* __restrict__ gw,
                                                const u16* __restrict__ w0, const u16* __restrict__ w1,
                                                const u16* __restrict__ w2, const u16* __restrict__ w3,
                                                char* ws, u16* out) {
    __shared__ __attribute__((aligned(16))) char smem[L_END];
    LDSP char* sm = (LDSP char*)smem;
    const int tid = threadIdx.x, lane = tid & 63;
    const int wv = __builtin_amdgcn_readfirstlane(tid >> 6);
    const int p = wv >> 1, h = wv & 1;
    const int q = lane >> 4, n = lane & 15;
    const int id = blockIdx.x;
    const int s1 = (id & 7) * 32 + (id >> 3);
    const int r = id & 7, c = id >> 3;
    const bool xh = (c == 31) && (wv == 7);
    const f32x4 z4 = {0.f, 0.f, 0.f, 0.f};
    const __amdgpu_buffer_rsrc_t rs = __builtin_amdgcn_make_buffer_rsrc(ws, 0, WS_BYTES, 0x00020000);

    const int gch = lane ^ (((lane >> 4) & 1) << 1);
    {
        const u16* r0 = x + (size_t)(s1 + 256 * p) * 1024;
        const u16* r1 = r0 + (size_t)1024 * 1024;
        const u32 vo = gch * 16;
        const u32 l0 = (u32)(size_t)(sm + L_XS) + wv * 4096;
        u32 keep;
        asm volatile("s_mov_b32 %0, m0\n\ts_mov_b32 m0, %4\n\ts_nop 0\n\t"
                     "global_load_lds_dwordx4 %1, %2\n\tglobal_load_lds_dwordx4 %1, %2 offset:1024\n\t"
                     "s_add_u32 m0, m0, 2048\n\ts_nop 0\n\t"
                     "global_load_lds_dwordx4 %1, %3\n\tglobal_load_lds_dwordx4 %1, %3 offset:1024\n\t"
                     "s_mov_b32 m0, %0"
                     : "=&s"(keep) : "v"(vo), "s"(r0), "s"(r1), "s"(l0) : "memory", "scc");
    }
    PIN();
    const u16* wp = p == 0 ? w0 : p == 1 ? w1 : p == 2 ? w2 : w3;
    const __amdgpu_buffer_rsrc_t rw = __builtin_amdgcn_make_buffer_rsrc((void*)wp, 0, 2048 * 513 * 2, 0x00020000);
    const int wvo = ((r + 8 * ((lane >> 3) + 16 * ((lane >> 1) & 3) + 128 * h)) * 513 + 16 * c + 8 * (lane & 1)) * 2;
    u32x4 wl[4];
#define WLOAD(e) wl[e] = __builtin_amdgcn_raw_buffer_load_b128(rw, wvo, (8 * ((e) & 1) + 64 * ((e) >> 1)) * 8 * 513 * 2, 0)
    WLOAD(0);
    PIN();

    const int rho = 16 * h + n;
    const u32 mn = (n & 1) ? NEG2 : 0u;
    f32x4 tc[2], ts[2];
    float c4[4], s4[4];
#pragma unroll
    for (int i = 0; i < 4; ++i) {
        const float a = (float)((4 * q + i) * n) * (1.0f / 32.0f);
        c4[i] = cosr(a); s4[i] = sinr(a);
    }
#pragma unroll
    for (int T = 0; T < 2; ++T)
#pragma unroll
        for (int i = 0; i < 4; ++i) {
            const float a = (float)((16 * T + 4 * q + i) * rho) * (1.0f / 1024.0f);
            tc[T][i] = cosr(a); ts[T][i] = sinr(a);
        }
    u32x4 gw0, gw1;
    {
        const u32 c01 = pk(c4[0], c4[1]), c23 = pk(c4[2], c4[3]), s01 = pk(s4[0], s4[1]), s23 = pk(s4[2], s4[3]);
        const u32 mh = h ? 0x80000000u : 0u;
        u32x4 Cf = {c01 ^ mh, c23 ^ mh, c01 ^ mh ^ mn, c23 ^ mh ^ mn};
        u32x4 Sf = {s01 ^ mh, s23 ^ mh, s01 ^ mh ^ mn, s23 ^ mh ^ mn};
        u32x4 Nf = Sf ^ NEG2;
        u32x4 Are[2][2], Aim[2][2];
#pragma unroll
        for (int Tp = 0; Tp < 2; ++Tp)
#pragma unroll
            for (int T = 0; T < 2; ++T) {
                const u32 m = (Tp ? 0x80000000u : 0u) ^ (T ? mn : 0u);
                Are[Tp][T] = u32x4{c01 ^ m, c23 ^ m, s01 ^ m, s23 ^ m};
                Aim[Tp][T] = u32x4{s01 ^ m ^ NEG2, s23 ^ m ^ NEG2, c01 ^ m, c23 ^ m};
                KEEP(Are[Tp][T]); KEEP(Aim[Tp][T]);
            }
        u32x2 Sep;
        {
            const int rr = n >> 1, part = n & 1, sg = (rr & 1) ? -1 : 1;
            const float aa = (float)(q * rr + sg) * 0.125f, ab = (float)(q * rr - sg) * 0.125f;
            const float R2 = 1.41421356237f;
            const float ar = R2 * cosr(aa), ai = -R2 * sinr(aa), br = R2 * cosr(ab), bi = -R2 * sinr(ab);
            Sep[0] = part ? pk(ai, ar) : pk(ar, -ai);
            Sep[1] = part ? pk(bi, -br) : pk(br, bi);
        }
        KEEP(Cf); KEEP(Sf); KEEP(Nf); KEEP(Sep); KEEP(tc[0]); KEEP(tc[1]); KEEP(ts[0]); KEEP(ts[1]);
        const LDSP char* xs = sm + L_XS + wv * 4096;
        const LDSP char* xb0 = xs + 64 * (4 * q + (n >> 2)) + 32 * (q & 1) + 8 * (n & 3);
        const LDSP char* xb1 = xs + 64 * (4 * q + (n >> 2)) + 32 * ((q & 1) ^ 1) + 8 * (n & 3);
        LDSP char* zw = sm + L_ZS + p * ZP + rho * 128;
        PIN();
        WLOAD(1);
        PIN();
        asm volatile("s_waitcnt vmcnt(%0)" :: "n"(2 + WARMUP) : "memory");
        PIN();
        u32x4 Bd[2];
#pragma unroll
        for (int T = 0; T < 2; ++T) {
            const LDSP char* b = T ? xb1 : xb0;
            const u32x2 a0 = trd(b), a1 = trd(b + 1024), a2 = trd(b + 2048), a3 = trd(b + 3072);
            const u32x4 xr = {a0[0], a0[1], a1[0], a1[1]}, xi = {a2[0], a2[1], a3[0], a3[1]};
            f32x4 Dr = mma(xr, Cf, z4); Dr = mma(xi, Sf, Dr);
            f32x4 Di = mma(xr, Nf, z4); Di = mma(xi, Cf, Di);
            const f32x4 re = Dr * tc[T] + Di * ts[T], im = Di * tc[T] - Dr * ts[T];
            Bd[T] = u32x4{pk(re[0], re[1]), pk(re[2], re[3]), pk(im[0], im[1]), pk(im[2], im[3])};
        }
        WLOAD(2);
#pragma unroll
        for (int Tp = 0; Tp < 2; ++Tp) {
            f32x4 Zr = mma(Are[Tp][0], Bd[0], z4); Zr = mma(Are[Tp][1], Bd[1], Zr);
            f32x4 Zi = mma(Aim[Tp][0], Bd[0], z4); Zi = mma(Aim[Tp][1], Bd[1], Zi);
            const u32x4 o = pk4(Zr, Zi);
            *(LDSP u32x4*)(zw + (((4 * Tp + q) ^ (rho & 7)) << 4)) = o;
            if (rho == 0) {
                if (Tp == 1) {
#pragma unroll
                    for (int i = 0; i < 4; ++i) *(LDSP u32*)(zw + 32 * 128 + ((15 + 4 * q + i) << 2)) = o[i];
                } else if (q == 0) {
                    *(LDSP u32*)(zw + 32 * 128 + 31 * 4) = o[0];
                }
            }
        }
        BAR();
        {
            const int rm = 32 - rho;
            const LDSP char* zq = sm + L_ZS + q * ZP;
            const u32x4 dv = *(const LDSP u32x4*)(zq + rho * 128 + ((p ^ (rho & 7)) << 4));
            const u32x4 mv = *(const LDSP u32x4*)(zq + rm * 128 + (((7 - p) ^ (rm & 7)) << 4));
            const int vo = s1 * ROW + (n >> 1) * 4096 + (h + 8 * p) * 128 + q * 32 + (n & 1) * 16;
#pragma unroll
            for (int e = 0; e < 4; ++e) {
                const f32x4 d = mma16(u32x2{dv[e], mv[3 - e]}, Sep, z4);
                __builtin_amdgcn_raw_buffer_store_b128(u32x4{pk(d[0], d[1]), TAG, pk(d[2], d[3]), TAG}, rs, vo + e * 256, 0, SC1);
            }
            if (wv == 6) {
                const u32 v = *(const LDSP u32*)(zq + 64);
                const f32x4 d = mma16(u32x2{v, v}, Sep, z4);
                if (q == 0) __builtin_amdgcn_raw_buffer_store_b64(u32x2{pk(d[0], d[0]), TAG}, rs, s1 * ROW + 32768 + n * 8, 0, SC1);
            }
        }
        PIN();
        WLOAD(3);
        const __amdgpu_buffer_rsrc_t rg = __builtin_amdgcn_make_buffer_rsrc((void*)gw, 0, 2048, 0x00020000);
        gw0 = __builtin_amdgcn_raw_buffer_load_b128(rg, gch * 16, 0, 0);
        gw1 = __builtin_amdgcn_raw_buffer_load_b128(rg, gch * 16 + 1024, 0, 0);
        PIN();
    }
    u32 wx[4][4];
    if (wv < 6) {
        const bool ua = (wv == 1) || (wv == 2);
        const int be = wv == 0 ? 16 * r : (wv == 1 ? r : 0);
        const int ga = (wv == 1 || wv == 4) ? 8 : ((wv == 3 || wv == 5) ? -128 : 128);
        const int de = wv == 4 ? r : (wv == 5 ? -16 * r : 0);
        const float u = (float)(be + ga * n) * (1.0f / 2048.0f), w = (float)(de * n) * (1.0f / 2048.0f);
        f32x4 cc, ss;
#pragma unroll
        for (int i = 0; i < 4; ++i) {
            const int al = ua ? 8 * (i >> 1) + 2 * q + (i & 1) : 4 * q + i;
            const float a = (float)al * u + w;
            cc[i] = cosr(a); ss[i] = sinr(a);
        }
        u32x4 o0 = __builtin_bit_cast(u32x4, cc), o1 = __builtin_bit_cast(u32x4, ss);
        if (wv != 1 && wv != 4) { o0 = pk4(cc, ss); o1 = pk4(-ss, cc); }
        *(LDSP u32x4*)(sm + L_KC + wv * 2048 + lane * 16) = o0;
        *(LDSP u32x4*)(sm + L_KC + wv * 2048 + 1024 + lane * 16) = o1;
    } else if (xh) {
#pragma unroll
        for (int a = 0; a < 4; ++a) {
            const u16* wq = a == 0 ? w0 : a == 1 ? w1 : a == 2 ? w2 : w3;
            const __amdgpu_buffer_rsrc_t rq = __builtin_amdgcn_make_buffer_rsrc((void*)wq, 0, 2048 * 513 * 2, 0x00020000);
#pragma unroll
            for (int i = 0; i < 4; ++i)
                wx[a][i] = __builtin_amdgcn_raw_buffer_load_b16(rq, ((r + 8 * (n + 64 * q)) * 513 + 512) * 2, i * 128 * 513 * 2, 0);
        }
        PIN();
    }
    {
        LDSP char* wt = sm + L_WT + p * 8320 + (lane & 1) * 4160 + ((lane >> 3) * 4 + ((lane >> 1) & 3) + 128 * h) * 4;
#pragma unroll
        for (int e = 0; e < 4; ++e)
#pragma unroll
            for (int j = 0; j < 4; ++j)
                *(LDSP u32*)(wt + j * 1024 + (32 * (e & 1) + 64 * (e >> 1)) * 4) = wl[e][j];
    }
    const int lo2 = (32 * wv + (lane >> 3)) * ROW + (r * 32 + c) * 128;
    const int xo = ((8 * ((n >> 1) & 1) + 2 * (n >> 2) + (n & 1)) + 64 * q) * ROW + 32768 + r * 16;
    KEEP(gw0); KEEP(gw1);
    PIN();
    asm volatile("s_waitcnt vmcnt(0)" ::: "memory");
    PIN();
    K2C k;
    f32x4 W[4][2];
    u32x4 xa[4];
#pragma unroll
    for (int e = 0; e < 4; ++e) xa[e] = u32x4{TAG, TAG, TAG, TAG};
    {
        u32x4 a[4];
#pragma unroll
        for (int e = 0; e < 4; ++e) a[e] = __builtin_amdgcn_raw_buffer_load_b128(rs, lo2 + (lane & 7) * 16, e * 8 * ROW, SC1);
        if (xh) {
#pragma unroll
            for (int e = 0; e < 4; ++e) xa[e] = __builtin_amdgcn_raw_buffer_load_b128(rs, xo, e * 16 * ROW, SC1);
        }
        PIN();
        {
            const LDSP char* xs = sm + L_XS + wv * 4096 + 2048 * h + lane * 16;
            const u32x4 x0 = *(const LDSP u32x4*)(xs), x1 = *(const LDSP u32x4*)(xs + 1024);
            float acc = 0.f, acc2 = 0.f;
#pragma unroll
            for (int i = 0; i < 4; ++i) { acc = dot2(x0[i], gw0[i], acc); acc2 = dot2(x1[i], gw1[i], acc2); }
            acc += acc2;
            acc = dpp_add<0x111, 0xf>(acc); acc = dpp_add<0x112, 0xf>(acc); acc = dpp_add<0x114, 0xf>(acc); acc = dpp_add<0x118, 0xf>(acc);
            acc = dpp_add<0x142, 0xa>(acc); acc = dpp_add<0x143, 0xc>(acc);
            const float z = bfr(acc);
            const float g = bfr(__builtin_amdgcn_rcpf(1.0f + __expf(-z)));
            const float og = bfr(1.0f - g);
            if (lane == 63) {
                LDSP float* G = (LDSP float*)(sm + L_G) + p * 4;
                G[h] = g; G[2 + h] = og;
            }
        }
        BAR();
        {
            const LDSP char* wt = sm + L_WT + (wv >> 2) * 4160 + (wv & 3) * 1024 + lane * 16;
#pragma unroll
            for (int j = 0; j < 4; ++j) {
                const u32x4 v = *(const LDSP u32x4*)(wt + j * 8320);
                W[j][0] = f32x4{lo16(v[0]), lo16(v[1]), lo16(v[2]), lo16(v[3])};
                W[j][1] = f32x4{hi16(v[0]), hi16(v[1]), hi16(v[2]), hi16(v[3])};
                KEEP(W[j][0]); KEEP(W[j][1]);
            }
            const LDSP char* kc = sm + L_KC + lane * 16;
            k.F1r = *(const LDSP u32x4*)(kc); k.F1i = *(const LDSP u32x4*)(kc + 1024);
            k.t1c = *(const LDSP f32x4*)(kc + 2048); k.t1s = *(const LDSP f32x4*)(kc + 3072);
            k.F2r = *(const LDSP u32x4*)(kc + 4096); k.F2i = *(const LDSP u32x4*)(kc + 5120);
            k.G1r = *(const LDSP u32x4*)(kc + 6144); k.G1i = *(const LDSP u32x4*)(kc + 7168);
            k.t2c = *(const LDSP f32x4*)(kc + 8192); k.t2s = *(const LDSP f32x4*)(kc + 9216);
            k.G2r = *(const LDSP u32x4*)(kc + 10240); k.G2i = *(const LDSP u32x4*)(kc + 11264);
            KEEP(k.F1r); KEEP(k.F1i); KEEP(k.F2r); KEEP(k.F2i); KEEP(k.G1r); KEEP(k.G1i); KEEP(k.G2r); KEEP(k.G2i);
            KEEP(k.t1c); KEEP(k.t1s); KEEP(k.t2c); KEEP(k.t2s);
        }
        PIN();
        for (unsigned spins = 0;;) {
            u32 bad = 0;
#pragma unroll
            for (int e = 0; e < 4; ++e) bad |= (a[e][1] ^ TAG) | (a[e][3] ^ TAG);
            if (xh) {
#pragma unroll
                for (int e = 0; e < 4; ++e) bad |= (xa[e][1] ^ TAG) | (xa[e][3] ^ TAG);
            }
            if (__all(bad == 0) || ++spins > SPIN_CAP) break;
            __builtin_amdgcn_s_sleep(2);
#pragma unroll
            for (int e = 0; e < 4; ++e) a[e] = __builtin_amdgcn_raw_buffer_load_b128(rs, lo2 + (lane & 7) * 16, e * 8 * ROW, SC1);
            if (xh) {
#pragma unroll
                for (int e = 0; e < 4; ++e) xa[e] = __builtin_amdgcn_raw_buffer_load_b128(rs, xo, e * 16 * ROW, SC1);
            }
        }
#pragma unroll
        for (int e = 0; e < 4; ++e) KEEP(xa[e]);
        const int qd = (lane >> 1) & 3;
        LDSP char* t1 = sm + L_T1 + qd * 4224 + (qd & 1) * 16 + (qd >> 1) * 64 + h * 512 + p * 128 + (lane & 1) * 32 + (lane >> 3) * 4;
#pragma unroll
        for (int e = 0; e < 4; ++e) {
            *(LDSP u32*)(t1 + (e & 1) * 1024 + (e >> 1) * 64) = a[e][0];
            *(LDSP u32*)(t1 + 2048 + (e & 1) * 1024 + (e >> 1) * 64) = a[e][2];
        }
    }
    BAR();
    {
        const LDSP char* t1 = sm + L_T1 + p * 4224 + (p & 1) * 16 + (p >> 1) * 64 + h * 2048 + lane * 8;
        f32x4 dr[2], di[2];
#pragma unroll
        for (int t = 0; t < 2; ++t) {
            const u32x2 a0 = trd(t1 + t * 1024), a1 = trd(t1 + t * 1024 + 512);
            const u32x4 a = {a0[0], a0[1], a1[0], a1[1]};
            const f32x4 er = mma(a, k.F1r, z4), ei = mma(a, k.F1i, z4);
            const f32x4 c4v = {k.t1c[2 * t], k.t1c[2 * t], k.t1c[2 * t + 1], k.t1c[2 * t + 1]};
            const f32x4 s4v = {k.t1s[2 * t], k.t1s[2 * t], k.t1s[2 * t + 1], k.t1s[2 * t + 1]};
            dr[t] = er * c4v + ei * s4v; di[t] = ei * c4v - er * s4v;
        }
        u32x4 o[2][2];
#pragma unroll
        for (int u = 0; u < 2; ++u) {
            const u32x4 b = {pk(dr[0][u], di[0][u]), pk(dr[0][2 + u], di[0][2 + u]), pk(dr[1][u], di[1][u]), pk(dr[1][2 + u], di[1][2 + u])};
            const f32x4 Xr = mma(k.F2r, b, z4), Xi = mma(k.F2i, b, z4);
            o[0][u] = k2_inv(k, Xr, Xi, W[0][u], W[1][u]);
            o[1][u] = k2_inv(k, Xr, Xi, W[2][u], W[3][u]);
        }
        LDSP char* t2 = sm + L_T2 + (n + 64 * q) * 128 + ((wv ^ (n & 7)) << 4);
#pragma unroll
        for (int i = 0; i < 4; ++i)
            *(LDSP u32x4*)(t2 + i * 2048) = u32x4{o[0][0][i], o[1][0][i], o[0][1][i], o[1][1][i]};
        BAR();
        const LDSP char* t2r = sm + L_T2 + wv * 4096 + lane * 16;
        const int vo = lo2 + (((lane & 7) ^ (lane >> 3)) << 4);
#pragma unroll
        for (int e = 0; e < 4; ++e)
            __builtin_amdgcn_raw_buffer_store_b128(*(const LDSP u32x4*)(t2r + e * 1024), rs, vo, e * 8 * ROW, SC1);
    }
    if (xh) {
        u32x4 f;
#pragma unroll
        for (int e = 0; e < 4; ++e) f[e] = (xa[e][0] & 0xffffu) | (xa[e][2] & 0xffff0000u);
        const f32x4 er = mma(f, k.F1r, z4), ei = mma(f, k.F1i, z4);
        const u32x4 b = pk4(er * k.t1c + ei * k.t1s, ei * k.t1c - er * k.t1s);
        const f32x4 Xr = mma(k.F2r, b, z4), Xi = mma(k.F2i, b, z4);
        f32x4 wf[4];
#pragma unroll
        for (int j = 0; j < 4; ++j) wf[j] = f32x4{lo16(wx[j][0]), lo16(wx[j][1]), lo16(wx[j][2]), lo16(wx[j][3])};
        const u32x4 oa = k2_inv(k, Xr, Xi, wf[0], wf[1]), ob = k2_inv(k, Xr, Xi, wf[2], wf[3]);
        const int vs = (n + 64 * q) * ROW + 32768 + r * 16;
#pragma unroll
        for (int i = 0; i < 4; ++i)
            __builtin_amdgcn_raw_buffer_store_b128(u32x4{oa[i], ob[i], 0u, 0u}, rs, vs, i * 16 * ROW, SC1);
    }
    u32x4 Fr[2][2], Fi[2][2], Er_[2], Ei_[2], Mix;
    {
        u32 fr[4], fi[4];
#pragma unroll
        for (int i = 0; i < 4; ++i) { fr[i] = pk(c4[i], -s4[i]); fi[i] = pk(s4[i], c4[i]); }
#pragma unroll
        for (int Tp = 0; Tp < 2; ++Tp)
#pragma unroll
            for (int T = 0; T < 2; ++T) {
                const u32 m0 = T ? mn : 0u, m1 = m0 ^ (Tp ? NEG2 : 0u);
                Fr[Tp][T] = u32x4{fr[0] ^ m0, fr[1] ^ m1, fr[2] ^ m0, fr[3] ^ m1};
                Fi[Tp][T] = u32x4{fi[0] ^ m0, fi[1] ^ m1, fi[2] ^ m0, fi[3] ^ m1};
                KEEP(Fr[Tp][T]); KEEP(Fi[Tp][T]);
            }
#pragma unroll
        for (int s = 0; s < 2; ++s) {
            const u32 m0 = s ? mn : 0u, m1 = m0 ^ (h ? NEG2 : 0u);
            Er_[s] = u32x4{fr[0] ^ m0, fr[1] ^ m1, fr[2] ^ m0, fr[3] ^ m1};
            Ei_[s] = u32x4{fi[0] ^ m0, fi[1] ^ m1, fi[2] ^ m0, fi[3] ^ m1};
            KEEP(Er_[s]); KEEP(Ei_[s]);
        }
        const f32x4 G = *(const LDSP f32x4*)(sm + L_G + (n >> 2) * 16);
        const int pp = n >> 2, i = n & 3;
        const float sc = 1.0f / 4194304.0f;
#pragma unroll
        for (int rr = 0; rr < 2; ++rr) {
            const float a = (float)(pp * (2 * q + rr)) * 0.125f;
            const float cc = cosr(a) * sc, ss = sinr(a) * sc;
            const float tau = ((rr == 0) == (i < 2)) ? 1.f : -1.f;
#pragma unroll
            for (int f = 0; f < 2; ++f) {
                const float g0 = G[2 * f], g1 = tau * G[2 * f + 1];
                const float cr = cc * g0 - ss * g1, ci = ss * g0 + cc * g1;
                Mix[2 * rr + f] = (i & 1) ? (i == 1 ? pk(ci, cr) : pk(-ci, -cr)) : pk(cr, -ci);
            }
        }
        KEEP(Mix);
    }
    PIN();
    asm volatile("s_waitcnt vmcnt(0)" ::: "memory");
    PIN();
    {
        const int vo = s1 * ROW + (2 * q) * 4096 + (rho + 128 * p) * 8;
        const int vx = s1 * ROW + 32768 + q * 32;
        u32x2 a[8], b0 = {0u, 0u}, b1 = {0u, 0u};
        for (unsigned spins = 0;;) {
#pragma unroll
            for (int e = 0; e < 8; ++e) a[e] = __builtin_amdgcn_raw_buffer_load_b64(rs, vo + (e >> 1) * 256, (e & 1) * 4096, SC1);
            if (wv == 6) {
                b0 = __builtin_amdgcn_raw_buffer_load_b64(rs, vx, 0, SC1);
                b1 = __builtin_amdgcn_raw_buffer_load_b64(rs, vx + 16, 0, SC1);
            }
            bool ok = b0[1] != TAG && b1[1] != TAG;
#pragma unroll
            for (int e = 0; e < 8; ++e) ok = ok & (a[e][1] != TAG);
            if (__all(ok) || ++spins > SPIN_CAP) break;
            __builtin_amdgcn_s_sleep(2);
        }
        LDSP char* zq = sm + L_ZS + q * ZP;
        u32x4 dv, mv;
#pragma unroll
        for (int e = 0; e < 4; ++e) {
            f32x4 d = mma(Mix, u32x4{a[2 * e][0], a[2 * e][1], a[2 * e + 1][0], a[2 * e + 1][1]}, z4);
            if (e == 0 && wv == 0 && n == 0) { d[0] = 0.5f * (d[0] + d[2]); d[1] = 0.5f * (d[1] + d[3]); }
            dv[e] = pk(d[0], d[1]);
            mv[3 - e] = pk(d[2], d[3]);
        }
        const int rm = 32 - rho;
        *(LDSP u32x4*)(zq + rho * 128 + ((p ^ (rho & 7)) << 4)) = dv;
        *(LDSP u32x4*)(zq + rm * 128 + (((7 - p) ^ (rm & 7)) << 4)) = mv;
        if (rho == 0) {
#pragma unroll
            for (int e = 0; e < 4; ++e) {
                const int kk = 32 - 4 * p - e;
                if (kk < 32) *(LDSP u32*)(zq + kk * 4) = mv[3 - e];
            }
        }
        if (wv == 6) {
            const f32x4 d = mma(Mix, u32x4{b0[0], b0[1], b1[0], b1[1]}, z4);
            if (n == 0) *(LDSP u32*)(zq + 64) = pk(0.5f * (d[0] + d[2]), 0.5f * (d[1] + d[3]));
        }
    }
    BAR();
    {
        const LDSP char* zp = sm + L_ZS + p * ZP + n * 128;
        u32x4 Ad[2];
#pragma unroll
        for (int T = 0; T < 2; ++T) {
            const u32x4 a0 = *(const LDSP u32x4*)(zp + T * 2048 + ((q ^ (n & 7)) << 4));
            const u32x4 a1 = *(const LDSP u32x4*)(zp + T * 2048 + (((4 + q) ^ (n & 7)) << 4));
            f32x4 er = mma(a0, Er_[0], z4); er = mma(a1, Er_[1], er);
            f32x4 ei = mma(a0, Ei_[0], z4); ei = mma(a1, Ei_[1], ei);
            Ad[T] = pk4(er * tc[T] - ei * ts[T], ei * tc[T] + er * ts[T]);
        }
        const __amdgpu_buffer_rsrc_t ro = __builtin_amdgcn_make_buffer_rsrc(out, 0, 2048 * 1024 * 2, 0x00020000);
        const int vo = ((s1 + 256 * p) * 1024 + 16 * h + 4 * q + 32 * n) * 2;
#pragma unroll
        for (int Tp = 0; Tp < 2; ++Tp) {
            f32x4 orr = mma(Ad[0], Fr[Tp][0], z4); orr = mma(Ad[1], Fr[Tp][1], orr);
            f32x4 oi = mma(Ad[0], Fi[Tp][0], z4); oi = mma(Ad[1], Fi[Tp][1], oi);
            __builtin_amdgcn_raw_buffer_store_b64(u32x2{pk(orr[0], orr[1]), pk(orr[2], orr[3])}, ro, vo + 1024 * Tp, 0, OUTAUX);
            __builtin_amdgcn_raw_buffer_store_b64(u32x2{pk(oi[0], oi[1]), pk(oi[2], oi[3])}, ro, vo + 1024 * Tp, 2 << 20, OUTAUX);
        }
    }
    asm volatile(".Lgf_tail:");
}

extern "C" void kernel_launch(void* const* d_in, const int* in_sizes, int n_in,
                              void* d_out, int out_size, void* d_ws, size_t ws_size,
                              hipStream_t stream) {
    gf_fused<<<256, 512, 0, stream>>>((const u16*)d_in[0], (const u16*)d_in[6], (const u16*)d_in[1], (const u16*)d_in[2],
                                      (const u16*)d_in[4], (const u16*)d_in[5], (char*)d_ws, (u16*)d_out);
}
```

```cpp
#define WARMUP 0
#include <hip/hip_runtime.h>
#include <hip/hip_bf16.h>
#include <stdint.h>

typedef unsigned short u16;
typedef unsigned int u32;
typedef unsigned long long u64;
typedef __attribute__((ext_vector_type(8))) __bf16 bf16x8;
typedef __attribute__((ext_vector_type(2))) __bf16 bf16x2;
typedef __attribute__((ext_vector_type(4))) short s16x4;
typedef __attribute__((ext_vector_type(4))) float f32x4;
typedef __attribute__((ext_vector_type(2))) float f32x2;
typedef __attribute__((ext_vector_type(4))) unsigned u32x4;
typedef __attribute__((ext_vector_type(2))) unsigned u32x2;
#define LDSP __attribute__((address_space(3)))
#define DI __device__ __forceinline__

#define RT_(v) do { __builtin_amdgcn_sched_barrier(0); asm volatile("s_memrealtime %0\n\ts_waitcnt lgkmcnt(0)" : "=s"(v) :: "memory"); __builtin_amdgcn_sched_barrier(0);} while (0)
#define OUTAUX 0

DI u32 pk(float lo, float hi) {
    f32x2 t = {lo, hi};
    return __builtin_bit_cast(u32, __builtin_convertvector(t, bf16x2));
}
DI u32x4 pk4(f32x4 a, f32x4 b) { return u32x4{pk(a[0], b[0]), pk(a[1], b[1]), pk(a[2], b[2]), pk(a[3], b[3])}; }
DI float bfr(float v) { return (float)(__bf16)v; }
DI f32x4 mma(u32x4 a, u32x4 b, f32x4 c) {
    return __builtin_amdgcn_mfma_f32_16x16x32_bf16(__builtin_bit_cast(bf16x8, a), __builtin_bit_cast(bf16x8, b), c, 0, 0, 0);
}
DI f32x4 mma16(u32x2 a, u32x2 b, f32x4 c) {
    return __builtin_amdgcn_mfma_f32_16x16x16bf16_1k(__builtin_bit_cast(s16x4, a), __builtin_bit_cast(s16x4, b), c, 0, 0, 0);
}
DI float dot2(u32 a, u32 b, float c) {
    return __builtin_amdgcn_fdot2_f32_bf16(__builtin_bit_cast(bf16x2, a), __builtin_bit_cast(bf16x2, b), c, false);
}
DI float cosr(float x) { return __builtin_amdgcn_cosf(x); }
DI float sinr(float x) { return __builtin_amdgcn_sinf(x); }
DI float lo16(u32 v) { return __builtin_bit_cast(float, v << 16); }
DI float hi16(u32 v) { return __builtin_bit_cast(float, v & 0xffff0000u); }
DI u32x2 trd(const LDSP char* p) {
    return __builtin_bit_cast(u32x2, __builtin_amdgcn_ds_read_tr16_b64_v4i16((LDSP s16x4*)p));
}
template <int CTRL, int RMASK> DI float dpp_add(float v) {
    const int t = __builtin_amdgcn_update_dpp(0, __builtin_bit_cast(int, v), CTRL, RMASK, 0xf, false);
    return v + __builtin_bit_cast(float, t);
}
#define BAR() asm volatile("s_waitcnt lgkmcnt(0)\n\ts_barrier" ::: "memory")
#define PIN() __builtin_amdgcn_sched_barrier(0)
#define KEEP(x) asm volatile("" : "+v"(x))

static constexpr u32 TAG = 0xFFAB7F95u;
static constexpr int SC1 = 16;
static constexpr int ROW = 32768 + 384;
static constexpr int WS_BYTES = 256 * ROW;
static constexpr int ZP = 33 * 128;
static constexpr int L_XS = 0;
static constexpr int L_ZS = 32768;
static constexpr int L_WT = L_ZS + 4 * ZP;
static constexpr int L_T1 = L_WT + 4 * 8320;
static constexpr int L_T2 = L_T1 + 4 * 4224;
static constexpr int L_KC = L_T2 + 32768;
static constexpr int L_G = L_KC + 12288;
static constexpr int L_DUM = L_G + 64;
static constexpr int L_END = L_DUM + 256;
static constexpr unsigned SPIN_CAP = 1u << 16;
static constexpr u32 NEG2 = 0x80008000u;

struct K2C {
    u32x4 F1r, F1i, F2r, F2i, G1r, G1i, G2r, G2i;
    f32x4 t1c, t1s, t2c, t2s;
};

DI u32x4 k2_inv(const K2C& k, f32x4 Xr, f32x4 Xi, f32x4 wr, f32x4 wi) {
    const f32x4 z4 = {0.f, 0.f, 0.f, 0.f};
    const u32x4 y = pk4(Xr * wr - Xi * wi, Xr * wi + Xi * wr);
    const f32x4 er = mma(y, k.G1r, z4), ei = mma(y, k.G1i, z4);
    const u32x4 b = pk4(er * k.t2c - ei * k.t2s, ei * k.t2c + er * k.t2s);
    return pk4(mma(k.G2r, b, z4), mma(k.G2i, b, z4));
}

__global__ __launch_bounds__(512) void gf_fused(const u16* __restrict__ x, const u16* __restrict__ gw,
                                                const u16* __restrict__ w0, const u16* __restrict__ w1,
                                                const u16* __restrict__ w2, const u16* __restrict__ w3,
                                                char* ws, u16* out) {
    __shared__ __attribute__((aligned(16))) char smem[L_END];
    LDSP char* sm = (LDSP char*)smem;
    unsigned long long dt0_, dtj_ = 0; RT_(dt0_);
    const int tid = threadIdx.x, lane = tid & 63;
    const int wv = __builtin_amdgcn_readfirstlane(tid >> 6);
    const int p = wv >> 1, h = wv & 1;
    const int q = lane >> 4, n = lane & 15;
    const int id = blockIdx.x;
    const int s1 = (id & 7) * 32 + (id >> 3);
    const int r = id & 7, c = id >> 3;
    const bool xh = (c == 31) && (wv == 7);
    const f32x4 z4 = {0.f, 0.f, 0.f, 0.f};
    const __amdgpu_buffer_rsrc_t rs = __builtin_amdgcn_make_buffer_rsrc(ws, 0, WS_BYTES, 0x00020000);

    const int gch = lane ^ (((lane >> 4) & 1) << 1);
    {
        const u16* r0 = x + (size_t)(s1 + 256 * p) * 1024;
        const u16* r1 = r0 + (size_t)1024 * 1024;
        const u32 vo = gch * 16;
        const u32 l0 = (u32)(size_t)(sm + L_XS) + wv * 4096;
        u32 keep;
        asm volatile("s_mov_b32 %0, m0\n\ts_mov_b32 m0, %4\n\ts_nop 0\n\t"
                     "global_load_lds_dwordx4 %1, %2\n\tglobal_load_lds_dwordx4 %1, %2 offset:1024\n\t"
                     "s_add_u32 m0, m0, 2048\n\ts_nop 0\n\t"
                     "global_load_lds_dwordx4 %1, %3\n\tglobal_load_lds_dwordx4 %1, %3 offset:1024\n\t"
                     "s_mov_b32 m0, %0"
                     : "=&s"(keep) : "v"(vo), "s"(r0), "s"(r1), "s"(l0) : "memory", "scc");
    }
    PIN();
    const u16* wp = p == 0 ? w0 : p == 1 ? w1 : p == 2 ? w2 : w3;
    const __amdgpu_buffer_rsrc_t rw = __builtin_amdgcn_make_buffer_rsrc((void*)wp, 0, 2048 * 513 * 2, 0x00020000);
    const int wvo = ((r + 8 * ((lane >> 3) + 16 * ((lane >> 1) & 3) + 128 * h)) * 513 + 16 * c + 8 * (lane & 1)) * 2;
    u32x4 wl[4];
#define WLOAD(e) wl[e] = __builtin_amdgcn_raw_buffer_load_b128(rw, wvo, (8 * ((e) & 1) + 64 * ((e) >> 1)) * 8 * 513 * 2, 0)
    WLOAD(0);
    PIN();

    const int rho = 16 * h + n;
    const u32 mn = (n & 1) ? NEG2 : 0u;
    f32x4 tc[2], ts[2];
    float c4[4], s4[4];
#pragma unroll
    for (int i = 0; i < 4; ++i) {
        const float a = (float)((4 * q + i) * n) * (1.0f / 32.0f);
        c4[i] = cosr(a); s4[i] = sinr(a);
    }
#pragma unroll
    for (int T = 0; T < 2; ++T)
#pragma unroll
        for (int i = 0; i < 4; ++i) {
            const float a = (float)((16 * T + 4 * q + i) * rho) * (1.0f / 1024.0f);
            tc[T][i] = cosr(a); ts[T][i] = sinr(a);
        }
    u32x4 gw0, gw1;
    {
        const u32 c01 = pk(c4[0], c4[1]), c23 = pk(c4[2], c4[3]), s01 = pk(s4[0], s4[1]), s23 = pk(s4[2], s4[3]);
        const u32 mh = h ? 0x80000000u : 0u;
        u32x4 Cf = {c01 ^ mh, c23 ^ mh, c01 ^ mh ^ mn, c23 ^ mh ^ mn};
        u32x4 Sf = {s01 ^ mh, s23 ^ mh, s01 ^ mh ^ mn, s23 ^ mh ^ mn};
        u32x4 Nf = Sf ^ NEG2;
        u32x4 Are[2][2], Aim[2][2];
#pragma unroll
        for (int Tp = 0; Tp < 2; ++Tp)
#pragma unroll
            for (int T = 0; T < 2; ++T) {
                const u32 m = (Tp ? 0x80000000u : 0u) ^ (T ? mn : 0u);
                Are[Tp][T] = u32x4{c01 ^ m, c23 ^ m, s01 ^ m, s23 ^ m};
                Aim[Tp][T] = u32x4{s01 ^ m ^ NEG2, s23 ^ m ^ NEG2, c01 ^ m, c23 ^ m};
                KEEP(Are[Tp][T]); KEEP(Aim[Tp][T]);
            }
        u32x2 Sep;
        {
            const int rr = n >> 1, part = n & 1, sg = (rr & 1) ? -1 : 1;
            const float aa = (float)(q * rr + sg) * 0.125f, ab = (float)(q * rr - sg) * 0.125f;
            const float R2 = 1.41421356237f;
            const float ar = R2 * cosr(aa), ai = -R2 * sinr(aa), br = R2 * cosr(ab), bi = -R2 * sinr(ab);
            Sep[0] = part ? pk(ai, ar) : pk(ar, -ai);
            Sep[1] = part ? pk(bi, -br) : pk(br, bi);
        }
        KEEP(Cf); KEEP(Sf); KEEP(Nf); KEEP(Sep); KEEP(tc[0]); KEEP(tc[1]); KEEP(ts[0]); KEEP(ts[1]);
        const LDSP char* xs = sm + L_XS + wv * 4096;
        const LDSP char* xb0 = xs + 64 * (4 * q + (n >> 2)) + 32 * (q & 1) + 8 * (n & 3);
        const LDSP char* xb1 = xs + 64 * (4 * q + (n >> 2)) + 32 * ((q & 1) ^ 1) + 8 * (n & 3);
        LDSP char* zw = sm + L_ZS + p * ZP + rho * 128;
        PIN();
        WLOAD(1);
        PIN();
        asm volatile("s_waitcnt vmcnt(%0)" :: "n"(2 + WARMUP) : "memory");
        PIN();
    RT_(dtj_);
        u32x4 Bd[2];
#pragma unroll
        for (int T = 0; T < 2; ++T) {
            const LDSP char* b = T ? xb1 : xb0;
            const u32x2 a0 = trd(b), a1 = trd(b + 1024), a2 = trd(b + 2048), a3 = trd(b + 3072);
            const u32x4 xr = {a0[0], a0[1], a1[0], a1[1]}, xi = {a2[0], a2[1], a3[0], a3[1]};
            f32x4 Dr = mma(xr, Cf, z4); Dr = mma(xi, Sf, Dr);
            f32x4 Di = mma(xr, Nf, z4); Di = mma(xi, Cf, Di);
            const f32x4 re = Dr * tc[T] + Di * ts[T], im = Di * tc[T] - Dr * ts[T];
            Bd[T] = u32x4{pk(re[0], re[1]), pk(re[2], re[3]), pk(im[0], im[1]), pk(im[2], im[3])};
        }
        WLOAD(2);
#pragma unroll
        for (int Tp = 0; Tp < 2; ++Tp) {
            f32x4 Zr = mma(Are[Tp][0], Bd[0], z4); Zr = mma(Are[Tp][1], Bd[1], Zr);
            f32x4 Zi = mma(Aim[Tp][0], Bd[0], z4); Zi = mma(Aim[Tp][1], Bd[1], Zi);
            const u32x4 o = pk4(Zr, Zi);
            *(LDSP u32x4*)(zw + (((4 * Tp + q) ^ (rho & 7)) << 4)) = o;
            if (rho == 0) {
                if (Tp == 1) {
#pragma unroll
                    for (int i = 0; i < 4; ++i) *(LDSP u32*)(zw + 32 * 128 + ((15 + 4 * q + i) << 2)) = o[i];
                } else if (q == 0) {
                    *(LDSP u32*)(zw + 32 * 128 + 31 * 4) = o[0];
                }
            }
        }
        BAR();
        {
            const int rm = 32 - rho;
            const LDSP char* zq = sm + L_ZS + q * ZP;
            const u32x4 dv = *(const LDSP u32x4*)(zq + rho * 128 + ((p ^ (rho & 7)) << 4));
            const u32x4 mv = *(const LDSP u32x4*)(zq + rm * 128 + (((7 - p) ^ (rm & 7)) << 4));
            const int vo = s1 * ROW + (n >> 1) * 4096 + (h + 8 * p) * 128 + q * 32 + (n & 1) * 16;
#pragma unroll
            for (int e = 0; e < 4; ++e) {
                const f32x4 d = mma16(u32x2{dv[e], mv[3 - e]}, Sep, z4);
                __builtin_amdgcn_raw_buffer_store_b128(u32x4{pk(d[0], d[1]), TAG, pk(d[2], d[3]), TAG}, rs, vo + e * 256, 0, SC1);
            }
            if (wv == 6) {
                const u32 v = *(const LDSP u32*)(zq + 64);
                const f32x4 d = mma16(u32x2{v, v}, Sep, z4);
                if (q == 0) __builtin_amdgcn_raw_buffer_store_b64(u32x2{pk(d[0], d[0]), TAG}, rs, s1 * ROW + 32768 + n * 8, 0, SC1);
            }
        }
        PIN();
        WLOAD(3);
        const __amdgpu_buffer_rsrc_t rg = __builtin_amdgcn_make_buffer_rsrc((void*)gw, 0, 2048, 0x00020000);
        gw0 = __builtin_amdgcn_raw_buffer_load_b128(rg, gch * 16, 0, 0);
        gw1 = __builtin_amdgcn_raw_buffer_load_b128(rg, gch * 16 + 1024, 0, 0);
        PIN();
    }
    u32 wx[4][4];
    if (wv < 6) {
        const bool ua = (wv == 1) || (wv == 2);
        const int be = wv == 0 ? 16 * r : (wv == 1 ? r : 0);
        const int ga = (wv == 1 || wv == 4) ? 8 : ((wv == 3 || wv == 5) ? -128 : 128);
        const int de = wv == 4 ? r : (wv == 5 ? -16 * r : 0);
        const float u = (float)(be + ga * n) * (1.0f / 2048.0f), w = (float)(de * n) * (1.0f / 2048.0f);
        f32x4 cc, ss;
#pragma unroll
        for (int i = 0; i < 4; ++i) {
            const int al = ua ? 8 * (i >> 1) + 2 * q + (i & 1) : 4 * q + i;
            const float a = (float)al * u + w;
            cc[i] = cosr(a); ss[i] = sinr(a);
        }
        u32x4 o0 = __builtin_bit_cast(u32x4, cc), o1 = __builtin_bit_cast(u32x4, ss);
        if (wv != 1 && wv != 4) { o0 = pk4(cc, ss); o1 = pk4(-ss, cc); }
        *(LDSP u32x4*)(sm + L_KC + wv * 2048 + lane * 16) = o0;
        *(LDSP u32x4*)(sm + L_KC + wv * 2048 + 1024 + lane * 16) = o1;
    } else if (xh) {
#pragma unroll
        for (int a = 0; a < 4; ++a) {
            const u16* wq = a == 0 ? w0 : a == 1 ? w1 : a == 2 ? w2 : w3;
            const __amdgpu_buffer_rsrc_t rq = __builtin_amdgcn_make_buffer_rsrc((void*)wq, 0, 2048 * 513 * 2, 0x00020000);
#pragma unroll
            for (int i = 0; i < 4; ++i)
                wx[a][i] = __builtin_amdgcn_raw_buffer_load_b16(rq, ((r + 8 * (n + 64 * q)) * 513 + 512) * 2, i * 128 * 513 * 2, 0);
        }
        PIN();
    }
    {
        LDSP char* wt = sm + L_WT + p * 8320 + (lane & 1) * 4160 + ((lane >> 3) * 4 + ((lane >> 1) & 3) + 128 * h) * 4;
#pragma unroll
        for (int e = 0; e < 4; ++e)
#pragma unroll
            for (int j = 0; j < 4; ++j)
                *(LDSP u32*)(wt + j * 1024 + (32 * (e & 1) + 64 * (e >> 1)) * 4) = wl[e][j];
    }
    const int lo2 = (32 * wv + (lane >> 3)) * ROW + (r * 32 + c) * 128;
    const int xo = ((8 * ((n >> 1) & 1) + 2 * (n >> 2) + (n & 1)) + 64 * q) * ROW + 32768 + r * 16;
    KEEP(gw0); KEEP(gw1);
    PIN();
    asm volatile("s_waitcnt vmcnt(0)" ::: "memory");
    PIN();
    K2C k;
    f32x4 W[4][2];
    u32x4 xa[4];
#pragma unroll
    for (int e = 0; e < 4; ++e) xa[e] = u32x4{TAG, TAG, TAG, TAG};
    {
        u32x4 a[4];
#pragma unroll
        for (int e = 0; e < 4; ++e) a[e] = __builtin_amdgcn_raw_buffer_load_b128(rs, lo2 + (lane & 7) * 16, e * 8 * ROW, SC1);
        if (xh) {
#pragma unroll
            for (int e = 0; e < 4; ++e) xa[e] = __builtin_amdgcn_raw_buffer_load_b128(rs, xo, e * 16 * ROW, SC1);
        }
        PIN();
        {
            const LDSP char* xs = sm + L_XS + wv * 4096 + 2048 * h + lane * 16;
            const u32x4 x0 = *(const LDSP u32x4*)(xs), x1 = *(const LDSP u32x4*)(xs + 1024);
            float acc = 0.f, acc2 = 0.f;
#pragma unroll
            for (int i = 0; i < 4; ++i) { acc = dot2(x0[i], gw0[i], acc); acc2 = dot2(x1[i], gw1[i], acc2); }
            acc += acc2;
            acc = dpp_add<0x111, 0xf>(acc); acc = dpp_add<0x112, 0xf>(acc); acc = dpp_add<0x114, 0xf>(acc); acc = dpp_add<0x118, 0xf>(acc);
            acc = dpp_add<0x142, 0xa>(acc); acc = dpp_add<0x143, 0xc>(acc);
            const float z = bfr(acc);
            const float g = bfr(__builtin_amdgcn_rcpf(1.0f + __expf(-z)));
            const float og = bfr(1.0f - g);
            if (lane == 63) {
                LDSP float* G = (LDSP float*)(sm + L_G) + p * 4;
                G[h] = g; G[2 + h] = og;
            }
        }
        BAR();
        {
            const LDSP char* wt = sm + L_WT + (wv >> 2) * 4160 + (wv & 3) * 1024 + lane * 16;
#pragma unroll
            for (int j = 0; j < 4; ++j) {
                const u32x4 v = *(const LDSP u32x4*)(wt + j * 8320);
                W[j][0] = f32x4{lo16(v[0]), lo16(v[1]), lo16(v[2]), lo16(v[3])};
                W[j][1] = f32x4{hi16(v[0]), hi16(v[1]), hi16(v[2]), hi16(v[3])};
                KEEP(W[j][0]); KEEP(W[j][1]);
            }
            const LDSP char* kc = sm + L_KC + lane * 16;
            k.F1r = *(const LDSP u32x4*)(kc); k.F1i = *(const LDSP u32x4*)(kc + 1024);
            k.t1c = *(const LDSP f32x4*)(kc + 2048); k.t1s = *(const LDSP f32x4*)(kc + 3072);
            k.F2r = *(const LDSP u32x4*)(kc + 4096); k.F2i = *(const LDSP u32x4*)(kc + 5120);
            k.G1r = *(const LDSP u32x4*)(kc + 6144); k.G1i = *(const LDSP u32x4*)(kc + 7168);
            k.t2c = *(const LDSP f32x4*)(kc + 8192); k.t2s = *(const LDSP f32x4*)(kc + 9216);
            k.G2r = *(const LDSP u32x4*)(kc + 10240); k.G2i = *(const LDSP u32x4*)(kc + 11264);
            KEEP(k.F1r); KEEP(k.F1i); KEEP(k.F2r); KEEP(k.F2i); KEEP(k.G1r); KEEP(k.G1i); KEEP(k.G2r); KEEP(k.G2i);
            KEEP(k.t1c); KEEP(k.t1s); KEEP(k.t2c); KEEP(k.t2s);
        }
        PIN();
        for (unsigned spins = 0;;) {
            u32 bad = 0;
#pragma unroll
            for (int e = 0; e < 4; ++e) bad |= (a[e][1] ^ TAG) | (a[e][3] ^ TAG);
            if (xh) {
#pragma unroll
                for (int e = 0; e < 4; ++e) bad |= (xa[e][1] ^ TAG) | (xa[e][3] ^ TAG);
            }
            if (__all(bad == 0) || ++spins > SPIN_CAP) break;
            __builtin_amdgcn_s_sleep(2);
#pragma unroll
            for (int e = 0; e < 4; ++e) a[e] = __builtin_amdgcn_raw_buffer_load_b128(rs, lo2 + (lane & 7) * 16, e * 8 * ROW, SC1);
            if (xh) {
#pragma unroll
                for (int e = 0; e < 4; ++e) xa[e] = __builtin_amdgcn_raw_buffer_load_b128(rs, xo, e * 16 * ROW, SC1);
            }
        }
#pragma unroll
        for (int e = 0; e < 4; ++e) KEEP(xa[e]);
        const int qd = (lane >> 1) & 3;
        LDSP char* t1 = sm + L_T1 + qd * 4224 + (qd & 1) * 16 + (qd >> 1) * 64 + h * 512 + p * 128 + (lane & 1) * 32 + (lane >> 3) * 4;
#pragma unroll
        for (int e = 0; e < 4; ++e) {
            *(LDSP u32*)(t1 + (e & 1) * 1024 + (e >> 1) * 64) = a[e][0];
            *(LDSP u32*)(t1 + 2048 + (e & 1) * 1024 + (e >> 1) * 64) = a[e][2];
        }
    }
    BAR();
    {
        const LDSP char* t1 = sm + L_T1 + p * 4224 + (p & 1) * 16 + (p >> 1) * 64 + h * 2048 + lane * 8;
        f32x4 dr[2], di[2];
#pragma unroll
        for (int t = 0; t < 2; ++t) {
            const u32x2 a0 = trd(t1 + t * 1024), a1 = trd(t1 + t * 1024 + 512);
            const u32x4 a = {a0[0], a0[1], a1[0], a1[1]};
            const f32x4 er = mma(a, k.F1r, z4), ei = mma(a, k.F1i, z4);
            const f32x4 c4v = {k.t1c[2 * t], k.t1c[2 * t], k.t1c[2 * t + 1], k.t1c[2 * t + 1]};
            const f32x4 s4v = {k.t1s[2 * t], k.t1s[2 * t], k.t1s[2 * t + 1], k.t1s[2 * t + 1]};
            dr[t] = er * c4v + ei * s4v; di[t] = ei * c4v - er * s4v;
        }
        u32x4 o[2][2];
#pragma unroll
        for (int u = 0; u < 2; ++u) {
            const u32x4 b = {pk(dr[0][u], di[0][u]), pk(dr[0][2 + u], di[0][2 + u]), pk(dr[1][u], di[1][u]), pk(dr[1][2 + u], di[1][2 + u])};
            const f32x4 Xr = mma(k.F2r, b, z4), Xi = mma(k.F2i, b, z4);
            o[0][u] = k2_inv(k, Xr, Xi, W[0][u], W[1][u]);
            o[1][u] = k2_inv(k, Xr, Xi, W[2][u], W[3][u]);
        }
        LDSP char* t2 = sm + L_T2 + (n + 64 * q) * 128 + ((wv ^ (n & 7)) << 4);
#pragma unroll
        for (int i = 0; i < 4; ++i)
            *(LDSP u32x4*)(t2 + i * 2048) = u32x4{o[0][0][i], o[1][0][i], o[0][1][i], o[1][1][i]};
        BAR();
        const LDSP char* t2r = sm + L_T2 + wv * 4096 + lane * 16;
        const int vo = lo2 + (((lane & 7) ^ (lane >> 3)) << 4);
#pragma unroll
        for (int e = 0; e < 4; ++e)
            __builtin_amdgcn_raw_buffer_store_b128(*(const LDSP u32x4*)(t2r + e * 1024), rs, vo, e * 8 * ROW, SC1);
    }
    if (xh) {
        u32x4 f;
#pragma unroll
        for (int e = 0; e < 4; ++e) f[e] = (xa[e][0] & 0xffffu) | (xa[e][2] & 0xffff0000u);
        const f32x4 er = mma(f, k.F1r, z4), ei = mma(f, k.F1i, z4);
        const u32x4 b = pk4(er * k.t1c + ei * k.t1s, ei * k.t1c - er * k.t1s);
        const f32x4 Xr = mma(k.F2r, b, z4), Xi = mma(k.F2i, b, z4);
        f32x4 wf[4];
#pragma unroll
        for (int j = 0; j < 4; ++j) wf[j] = f32x4{lo16(wx[j][0]), lo16(wx[j][1]), lo16(wx[j][2]), lo16(wx[j][3])};
        const u32x4 oa = k2_inv(k, Xr, Xi, wf[0], wf[1]), ob = k2_inv(k, Xr, Xi, wf[2], wf[3]);
        const int vs = (n + 64 * q) * ROW + 32768 + r * 16;
#pragma unroll
        for (int i = 0; i < 4; ++i)
            __builtin_amdgcn_raw_buffer_store_b128(u32x4{oa[i], ob[i], 0u, 0u}, rs, vs, i * 16 * ROW, SC1);
    }
    u32x4 Fr[2][2], Fi[2][2], Er_[2], Ei_[2], Mix;
    {
        u32 fr[4], fi[4];
#pragma unroll
        for (int i = 0; i < 4; ++i) { fr[i] = pk(c4[i], -s4[i]); fi[i] = pk(s4[i], c4[i]); }
#pragma unroll
        for (int Tp = 0; Tp < 2; ++Tp)
#pragma unroll
            for (int T = 0; T < 2; ++T) {
                const u32 m0 = T ? mn : 0u, m1 = m0 ^ (Tp ? NEG2 : 0u);
                Fr[Tp][T] = u32x4{fr[0] ^ m0, fr[1] ^ m1, fr[2] ^ m0, fr[3] ^ m1};
                Fi[Tp][T] = u32x4{fi[0] ^ m0, fi[1] ^ m1, fi[2] ^ m0, fi[3] ^ m1};
                KEEP(Fr[Tp][T]); KEEP(Fi[Tp][T]);
            }
#pragma unroll
        for (int s = 0; s < 2; ++s) {
            const u32 m0 = s ? mn : 0u, m1 = m0 ^ (h ? NEG2 : 0u);
            Er_[s] = u32x4{fr[0] ^ m0, fr[1] ^ m1, fr[2] ^ m0, fr[3] ^ m1};
            Ei_[s] = u32x4{fi[0] ^ m0, fi[1] ^ m1, fi[2] ^ m0, fi[3] ^ m1};
            KEEP(Er_[s]); KEEP(Ei_[s]);
        }
        const f32x4 G = *(const LDSP f32x4*)(sm + L_G + (n >> 2) * 16);
        const int pp = n >> 2, i = n & 3;
        const float sc = 1.0f / 4194304.0f;
#pragma unroll
        for (int rr = 0; rr < 2; ++rr) {
            const float a = (float)(pp * (2 * q + rr)) * 0.125f;
            const float cc = cosr(a) * sc, ss = sinr(a) * sc;
            const float tau = ((rr == 0) == (i < 2)) ? 1.f : -1.f;
#pragma unroll
            for (int f = 0; f < 2; ++f) {
                const float g0 = G[2 * f], g1 = tau * G[2 * f + 1];
                const float cr = cc * g0 - ss * g1, ci = ss * g0 + cc * g1;
                Mix[2 * rr + f] = (i & 1) ? (i == 1 ? pk(ci, cr) : pk(-ci, -cr)) : pk(cr, -ci);
            }
        }
        KEEP(Mix);
    }
    PIN();
    asm volatile("s_waitcnt vmcnt(0)" ::: "memory");
    PIN();
    {
        const int vo = s1 * ROW + (2 * q) * 4096 + (rho + 128 * p) * 8;
        const int vx = s1 * ROW + 32768 + q * 32;
        u32x2 a[8], b0 = {0u, 0u}, b1 = {0u, 0u};
        for (unsigned spins = 0;;) {
#pragma unroll
            for (int e = 0; e < 8; ++e) a[e] = __builtin_amdgcn_raw_buffer_load_b64(rs, vo + (e >> 1) * 256, (e & 1) * 4096, SC1);
            if (wv == 6) {
                b0 = __builtin_amdgcn_raw_buffer_load_b64(rs, vx, 0, SC1);
                b1 = __builtin_amdgcn_raw_buffer_load_b64(rs, vx + 16, 0, SC1);
            }
            bool ok = b0[1] != TAG && b1[1] != TAG;
#pragma unroll
            for (int e = 0; e < 8; ++e) ok = ok & (a[e][1] != TAG);
            if (__all(ok) || ++spins > SPIN_CAP) break;
            __builtin_amdgcn_s_sleep(2);
        }
        LDSP char* zq = sm + L_ZS + q * ZP;
        u32x4 dv, mv;
#pragma unroll
        for (int e = 0; e < 4; ++e) {
            f32x4 d = mma(Mix, u32x4{a[2 * e][0], a[2 * e][1], a[2 * e + 1][0], a[2 * e + 1][1]}, z4);
            if (e == 0 && wv == 0 && n == 0) { d[0] = 0.5f * (d[0] + d[2]); d[1] = 0.5f * (d[1] + d[3]); }
            dv[e] = pk(d[0], d[1]);
            mv[3 - e] = pk(d[2], d[3]);
        }
        const int rm = 32 - rho;
        *(LDSP u32x4*)(zq + rho * 128 + ((p ^ (rho & 7)) << 4)) = dv;
        *(LDSP u32x4*)(zq + rm * 128 + (((7 - p) ^ (rm & 7)) << 4)) = mv;
        if (rho == 0) {
#pragma unroll
            for (int e = 0; e < 4; ++e) {
                const int kk = 32 - 4 * p - e;
                if (kk < 32) *(LDSP u32*)(zq + kk * 4) = mv[3 - e];
            }
        }
        if (wv == 6) {
            const f32x4 d = mma(Mix, u32x4{b0[0], b0[1], b1[0], b1[1]}, z4);
            if (n == 0) *(LDSP u32*)(zq + 64) = pk(0.5f * (d[0] + d[2]), 0.5f * (d[1] + d[3]));
        }
    }
    BAR();
    {
        const LDSP char* zp = sm + L_ZS + p * ZP + n * 128;
        u32x4 Ad[2];
#pragma unroll
        for (int T = 0; T < 2; ++T) {
            const u32x4 a0 = *(const LDSP u32x4*)(zp + T * 2048 + ((q ^ (n & 7)) << 4));
            const u32x4 a1 = *(const LDSP u32x4*)(zp + T * 2048 + (((4 + q) ^ (n & 7)) << 4));
            f32x4 er = mma(a0, Er_[0], z4); er = mma(a1, Er_[1], er);
            f32x4 ei = mma(a0, Ei_[0], z4); ei = mma(a1, Ei_[1], ei);
            Ad[T] = pk4(er * tc[T] - ei * ts[T], ei * tc[T] + er * ts[T]);
        }
        const __amdgpu_buffer_rsrc_t ro = __builtin_amdgcn_make_buffer_rsrc(out, 0, 2048 * 1024 * 2, 0x00020000);
        const int vo = ((s1 + 256 * p) * 1024 + 16 * h + 4 * q + 32 * n) * 2;
#pragma unroll
        for (int Tp = 0; Tp < 2; ++Tp) {
            f32x4 orr = mma(Ad[0], Fr[Tp][0], z4); orr = mma(Ad[1], Fr[Tp][1], orr);
            f32x4 oi = mma(Ad[0], Fi[Tp][0], z4); oi = mma(Ad[1], Fi[Tp][1], oi);
            __builtin_amdgcn_raw_buffer_store_b64(u32x2{pk(orr[0], orr[1]), pk(orr[2], orr[3])}, ro, vo + 1024 * Tp, 0, OUTAUX);
            __builtin_amdgcn_raw_buffer_store_b64(u32x2{pk(oi[0], oi[1]), pk(oi[2], oi[3])}, ro, vo + 1024 * Tp, 2 << 20, OUTAUX);
        }
    }
    asm volatile(".Lgf_tail:");
    asm volatile("s_waitcnt vmcnt(0)" ::: "memory");
    { unsigned long long te_, tn_; RT_(te_); for (int i_ = 0; i_ < 100000; ++i_) { RT_(tn_); if (tn_ - te_ >= dtj_ - dt0_) break; __builtin_amdgcn_s_sleep(1); } }
}

extern "C" void kernel_launch(void* const* d_in, const int* in_sizes, int n_in,
                              void* d_out, int out_size, void* d_ws, size_t ws_size,
                              hipStream_t stream) {
    gf_fused<<<256, 512, 0, stream>>>((const u16*)d_in[0], (const u16*)d_in[6], (const u16*)d_in[1], (const u16*)d_in[2],
                                      (const u16*)d_in[4], (const u16*)d_in[5], (char*)d_ws, (u16*)d_out);
}
```

```cpp
#include <hip/hip_runtime.h>
#include <hip/hip_bf16.h>
#include <stdint.h>

typedef unsigned short u16;
typedef unsigned int u32;
typedef unsigned long long u64;
typedef __attribute__((ext_vector_type(8))) __bf16 bf16x8;
typedef __attribute__((ext_vector_type(2))) __bf16 bf16x2;
typedef __attribute__((ext_vector_type(4))) short s16x4;
typedef __attribute__((ext_vector_type(4))) float f32x4;
typedef __attribute__((ext_vector_type(2))) float f32x2;
typedef __attribute__((ext_vector_type(4))) unsigned u32x4;
typedef __attribute__((ext_vector_type(2))) unsigned u32x2;
typedef __attribute__((ext_vector_type(3))) unsigned u32x3;
#define LDSP __attribute__((address_space(3)))
#define DI __device__ __forceinline__
#define WARMUP 0
#define EARLYC 1
#define NW0 4
#define NW1 6
#define NW2 8
#define DR1 0
#define DR2 0
#define MASKED 1
#define OUTAUX 0

DI u32 pk(float lo, float hi) {
    f32x2 t = {lo, hi};
    return __builtin_bit_cast(u32, __builtin_convertvector(t, bf16x2));
}
DI u32x4 pk4(f32x4 a, f32x4 b) { return u32x4{pk(a[0], b[0]), pk(a[1], b[1]), pk(a[2], b[2]), pk(a[3], b[3])}; }
DI float bfr(float v) { return (float)(__bf16)v; }
DI f32x4 mma(u32x4 a, u32x4 b, f32x4 c) {
    return __builtin_amdgcn_mfma_f32_16x16x32_bf16(__builtin_bit_cast(bf16x8, a), __builtin_bit_cast(bf16x8, b), c, 0, 0, 0);
}
DI f32x4 mma16(u32x2 a, u32x2 b, f32x4 c) {
    return __builtin_amdgcn_mfma_f32_16x16x16bf16_1k(__builtin_bit_cast(s16x4, a), __builtin_bit_cast(s16x4, b), c, 0, 0, 0);
}
DI float dot2(u32 a, u32 b, float c) {
    return __builtin_amdgcn_fdot2_f32_bf16(__builtin_bit_cast(bf16x2, a), __builtin_bit_cast(bf16x2, b), c, false);
}
DI float cosr(float x) { return __builtin_amdgcn_cosf(x); }
DI float sinr(float x) { return __builtin_amdgcn_sinf(x); }
DI float lo16(u32 v) { return __builtin_bit_cast(float, v << 16); }
DI float hi16(u32 v) { return __builtin_bit_cast(float, v & 0xffff0000u); }
DI u32x2 trd(const LDSP char* p) {
    return __builtin_bit_cast(u32x2, __builtin_amdgcn_ds_read_tr16_b64_v4i16((LDSP s16x4*)p));
}
template <int CTRL, int RMASK> DI float dpp_add(float v) {
    const int t = __builtin_amdgcn_update_dpp(0, __builtin_bit_cast(int, v), CTRL, RMASK, 0xf, false);
    return v + __builtin_bit_cast(float, t);
}
#define BAR() asm volatile("s_waitcnt lgkmcnt(0)\n\ts_barrier" ::: "memory")
#define PIN() __builtin_amdgcn_sched_barrier(0)
#define KEEP(x) asm volatile("" : "+v"(x))

static constexpr u32 TAG = 0xFFAB7F95u;
static constexpr int SC1 = 16;
static constexpr int ROW = 32768 + 128;
static constexpr int XOFF = 256 * ROW;
static constexpr int WS_BYTES = XOFF + 8 * 2048;
static constexpr int ZP = 33 * 128;
static constexpr int WA = 4 * 2080 + 1024;
static constexpr int L_XS = 0;
static constexpr int L_ZS = 16384;
static constexpr int L_WT = L_ZS + 4 * ZP;
static constexpr int L_T1 = L_WT + 4 * WA;
static constexpr int L_T2 = L_T1 + 4 * 4224;
static constexpr int L_KC = L_T2 + 32768;
static constexpr int L_G = L_KC + 12288;
static constexpr int L_DUM = L_G + 64;
static constexpr int L_END = L_DUM + 256;
static constexpr unsigned SPIN_CAP = 1u << 16;
static constexpr u32 NEG2 = 0x80008000u;

struct K2C {
    u32x4 F1r, F1i, F2r, F2i, G1r, G1i, G2r, G2i;
    f32x4 t1c, t1s, t2c, t2s;
};

DI void k2_read(K2C& k, const LDSP char* kc) {
    k.F1r = *(const LDSP u32x4*)(kc); k.F1i = *(const LDSP u32x4*)(kc + 1024);
    k.t1c = *(const LDSP f32x4*)(kc + 2048); k.t1s = *(const LDSP f32x4*)(kc + 3072);
    k.F2r = *(const LDSP u32x4*)(kc + 4096); k.F2i = *(const LDSP u32x4*)(kc + 5120);
    k.G1r = *(const LDSP u32x4*)(kc + 6144); k.G1i = *(const LDSP u32x4*)(kc + 7168);
    k.t2c = *(const LDSP f32x4*)(kc + 8192); k.t2s = *(const LDSP f32x4*)(kc + 9216);
    k.G2r = *(const LDSP u32x4*)(kc + 10240); k.G2i = *(const LDSP u32x4*)(kc + 11264);
    KEEP(k.F1r); KEEP(k.F1i); KEEP(k.F2r); KEEP(k.F2i); KEEP(k.G1r); KEEP(k.G1i); KEEP(k.G2r); KEEP(k.G2i);
    KEEP(k.t1c); KEEP(k.t1s); KEEP(k.t2c); KEEP(k.t2s);
}
DI u32x4 k2_inv(const K2C& k, f32x4 Xr, f32x4 Xi, f32x4 wr, f32x4 wi) {
    const f32x4 z4 = {0.f, 0.f, 0.f, 0.f};
    const u32x4 y = pk4(Xr * wr - Xi * wi, Xr * wi + Xi * wr);
    const f32x4 er = mma(y, k.G1r, z4), ei = mma(y, k.G1i, z4);
    const u32x4 b = pk4(er * k.t2c - ei * k.t2s, ei * k.t2c + er * k.t2s);
    return pk4(mma(k.G2r, b, z4), mma(k.G2i, b, z4));
}

__global__ __launch_bounds__(576) void gf_fused(const u16* __restrict__ x, const u16* __restrict__ gw,
                                                const u16* __restrict__ w0, const u16* __restrict__ w1,
                                                const u16* __restrict__ w2, const u16* __restrict__ w3,
                                                char* ws, u16* out) {
    __shared__ __attribute__((aligned(16))) char smem[L_END];
    LDSP char* sm = (LDSP char*)smem;
    const int tid = threadIdx.x, lane = tid & 63;
    const int wv = __builtin_amdgcn_readfirstlane(tid >> 6);
    const int p = wv >> 1, h = wv & 1;
    const int q = lane >> 4, n = lane & 15;
    const int id = blockIdx.x;
    const int s1 = (id & 7) * 32 + (id >> 3);
    const int r = id & 7, c = id >> 3;
    const f32x4 z4 = {0.f, 0.f, 0.f, 0.f};
    const __amdgpu_buffer_rsrc_t rs = __builtin_amdgcn_make_buffer_rsrc(ws, 0, WS_BYTES, 0x00020000);

    if (wv == 8) {
        if (c != 31) return;
        u32 last[4];
#pragma unroll
        for (int j = 0; j < 4; ++j) last[j] = *(const u32*)((j == 0 ? w0 : j == 1 ? w1 : j == 2 ? w2 : w3) + 2048 * 513 - 2);
        BAR(); BAR(); BAR();
        const int xo = XOFF + r * 2048 + ((8 * ((n >> 1) & 1) + 2 * (n >> 2) + (n & 1)) + 16 * q) * 32;
        u32x4 xa[2];
#pragma unroll
        for (int e = 0; e < 2; ++e) xa[e] = __builtin_amdgcn_raw_buffer_load_b128(rs, xo + e * 16, 0, SC1);
        PIN();
        K2C k;
        k2_read(k, sm + L_KC + lane * 16);
        f32x4 wf[4];
#pragma unroll
        for (int j = 0; j < 4; ++j) {
            const u32x4 v = *(const LDSP u32x4*)(sm + L_WT + j * WA + 8320 + lane * 16);
            wf[j] = f32x4{lo16(v[0]), lo16(v[1]), lo16(v[2]), lo16(v[3])};
            if (r == 7 && lane == 63) wf[j][3] = hi16(last[j]);
            KEEP(wf[j]);
        }
        BAR();
        for (unsigned spins = 0;;) {
            u32 bad = 0;
#pragma unroll
            for (int e = 0; e < 2; ++e) bad |= (xa[e][1] ^ TAG) | (xa[e][3] ^ TAG);
            if (__all(bad == 0) || ++spins > SPIN_CAP) break;
#pragma unroll
            for (int e = 0; e < 2; ++e) xa[e] = __builtin_amdgcn_raw_buffer_load_b128(rs, xo + e * 16, 0, SC1);
        }
        const u32x4 f = {xa[0][0], xa[0][2], xa[1][0], xa[1][2]};
        const f32x4 er = mma(f, k.F1r, z4), ei = mma(f, k.F1i, z4);
        const u32x4 b = pk4(er * k.t1c + ei * k.t1s, ei * k.t1c - er * k.t1s);
        const f32x4 Xr = mma(k.F2r, b, z4), Xi = mma(k.F2i, b, z4);
        const u32x4 oa = k2_inv(k, Xr, Xi, wf[0], wf[1]), ob = k2_inv(k, Xr, Xi, wf[2], wf[3]);
        const int vs = XOFF + r * 2048 + lane * 32;
        __builtin_amdgcn_raw_buffer_store_b128(u32x4{oa[0], ob[0], oa[1], ob[1]}, rs, vs, 0, SC1);
        __builtin_amdgcn_raw_buffer_store_b128(u32x4{oa[2], ob[2], oa[3], ob[3]}, rs, vs + 16, 0, SC1);
        BAR();
        return;
    }
    const int xpos = XOFF + (((s1 & 15) + 16 * (s1 >> 6)) * 4 + ((s1 >> 4) & 3)) * 8;
    const int rho = 16 * h + n;
    const u32 mn = (n & 1) ? NEG2 : 0u;
    float c4[4], s4[4];
#pragma unroll
    for (int i = 0; i < 4; ++i) {
        const float a = (float)((4 * q + i) * n) * (1.0f / 32.0f);
        c4[i] = cosr(a); s4[i] = sinr(a);
        KEEP(c4[i]); KEEP(s4[i]);
    }
    PIN();
    const int gch = lane ^ (((lane >> 4) & 1) << 1);
    const u16* wp = p == 0 ? w0 : p == 1 ? w1 : p == 2 ? w2 : w3;
    {
        const u16* r0 = x + (size_t)(s1 + 256 * wv) * 1024;
        const u32 vo = gch * 16;
        const u32 l0 = (u32)(size_t)(sm + L_XS) + wv * 2048;
        u32 keep;
        asm volatile("s_mov_b32 %0, m0\n\ts_mov_b32 m0, %3\n\ts_nop 0\n\t"
                     "global_load_lds_dwordx4 %1, %2\n\tglobal_load_lds_dwordx4 %1, %2 offset:1024\n\t"
                     "s_mov_b32 m0, %0"
                     : "=&s"(keep) : "v"(vo), "s"(r0), "s"(l0) : "memory");
    }
    PIN();

    const __amdgpu_buffer_rsrc_t rw = __builtin_amdgcn_make_buffer_rsrc((void*)wp, 0, 2048 * 513 * 2, 0x00020000);
    const int wvo = ((r + 8 * (q + 16 * ((lane >> 2) & 3) + 128 * h)) * 513 + 16 * c + 4 * (lane & 3)) * 2;
    u32x3 wl[8];
#define WLOADS(lo, hi) do { PIN(); \
        if (c == 31) { _Pragma("unroll") for (int e = lo; e < hi; ++e) \
            wl[e] = __builtin_amdgcn_raw_buffer_load_b96(rw, wvo, (4 * (e & 3) + 64 * (e >> 2)) * 8 * 513 * 2, 0); } \
        else { _Pragma("unroll") for (int e = lo; e < hi; ++e) { \
            const u32x2 t_ = __builtin_amdgcn_raw_buffer_load_b64(rw, wvo, (4 * (e & 3) + 64 * (e >> 2)) * 8 * 513 * 2, 0); \
            wl[e][0] = t_[0]; wl[e][1] = t_[1]; } } \
        PIN(); } while (0)
    WLOADS(0, NW0);

    f32x4 tc[2], ts[2];
#pragma unroll
    for (int T = 0; T < 2; ++T)
#pragma unroll
        for (int i = 0; i < 4; ++i) {
            const float a = (float)((16 * T + 4 * q + i) * rho) * (1.0f / 1024.0f);
            tc[T][i] = cosr(a); ts[T][i] = sinr(a);
        }
    u32x4 gw0, gw1;
    int zdo = L_ZS + q * ZP + rho * 128 + ((p ^ (rho & 7)) << 4);
    int zmo = L_ZS + q * ZP + (32 - rho) * 128 + (((7 - p) ^ ((32 - rho) & 7)) << 4);
    {
        const u32 c01 = pk(c4[0], c4[1]), c23 = pk(c4[2], c4[3]), s01 = pk(s4[0], s4[1]), s23 = pk(s4[2], s4[3]);
        const u32 mh = h ? 0x80000000u : 0u;
        u32x4 Cf = {c01 ^ mh, c23 ^ mh, c01 ^ mh ^ mn, c23 ^ mh ^ mn};
        u32x4 Sf = {s01 ^ mh, s23 ^ mh, s01 ^ mh ^ mn, s23 ^ mh ^ mn};
        u32x4 Nf = Sf ^ NEG2;
        u32x4 Are[2][2], Aim[2][2];
#pragma unroll
        for (int Tp = 0; Tp < 2; ++Tp)
#pragma unroll
            for (int T = 0; T < 2; ++T) {
                const u32 m = (Tp ? 0x80000000u : 0u) ^ (T ? mn : 0u);
                Are[Tp][T] = u32x4{c01 ^ m, c23 ^ m, s01 ^ m, s23 ^ m};
                Aim[Tp][T] = u32x4{s01 ^ m ^ NEG2, s23 ^ m ^ NEG2, c01 ^ m, c23 ^ m};
                KEEP(Are[Tp][T]); KEEP(Aim[Tp][T]);
            }
        u32x2 Sep;
        {
            const int rr = n >> 1, part = n & 1, sg = (rr & 1) ? -1 : 1;
            const float aa = (float)(q * rr + sg) * 0.125f, ab = (float)(q * rr - sg) * 0.125f;
            const float R2 = 1.41421356237f;
            const float ar = R2 * cosr(aa), ai = -R2 * sinr(aa), br = R2 * cosr(ab), bi = -R2 * sinr(ab);
            Sep[0] = part ? pk(ai, ar) : pk(ar, -ai);
            Sep[1] = part ? pk(bi, -br) : pk(br, bi);
        }
        KEEP(Cf); KEEP(Sf); KEEP(Nf); KEEP(Sep); KEEP(tc[0]); KEEP(tc[1]); KEEP(ts[0]); KEEP(ts[1]);
        const LDSP char* xs = sm + L_XS + p * 2048;
        const LDSP char* xb0 = xs + 64 * (4 * q + (n >> 2)) + 32 * (q & 1) + 8 * (n & 3);
        const LDSP char* xb1 = xs + 64 * (4 * q + (n >> 2)) + 32 * ((q & 1) ^ 1) + 8 * (n & 3);
        LDSP char* zw = sm + L_ZS + p * ZP + rho * 128;
        int vo = s1 * ROW + (n >> 1) * 4096 + (h + 8 * p) * 128 + q * 32 + (n & 1) * 16;
        u32x4 g1[4];
#pragma unroll
        for (int e = 0; e < 4; ++e) { g1[e] = u32x4{0u, TAG, 0u, TAG}; KEEP(g1[e]); }
        KEEP(vo); KEEP(zdo); KEEP(zmo);
        WLOADS(NW0, NW1);
        asm volatile("s_waitcnt vmcnt(%0)\n\ts_barrier" :: "n"(NW1 + WARMUP) : "memory");
        PIN();
        u32x4 Bd[2];
#pragma unroll
        for (int T = 0; T < 2; ++T) {
            const LDSP char* b = T ? xb1 : xb0;
            const u32x2 a0 = trd(b), a1 = trd(b + 1024), a2 = trd(b + 8192), a3 = trd(b + 9216);
            const u32x4 xr = {a0[0], a0[1], a1[0], a1[1]}, xi = {a2[0], a2[1], a3[0], a3[1]};
            f32x4 Dr = mma(xr, Cf, z4); Dr = mma(xi, Sf, Dr);
            f32x4 Di = mma(xr, Nf, z4); Di = mma(xi, Cf, Di);
            const f32x4 re = Dr * tc[T] + Di * ts[T], im = Di * tc[T] - Dr * ts[T];
            Bd[T] = u32x4{pk(re[0], re[1]), pk(re[2], re[3]), pk(im[0], im[1]), pk(im[2], im[3])};
        }
        WLOADS(NW1, NW2);
#pragma unroll
        for (int Tp = 0; Tp < 2; ++Tp) {
            f32x4 Zr = mma(Are[Tp][0], Bd[0], z4); Zr = mma(Are[Tp][1], Bd[1], Zr);
            f32x4 Zi = mma(Aim[Tp][0], Bd[0], z4); Zi = mma(Aim[Tp][1], Bd[1], Zi);
            const u32x4 o = pk4(Zr, Zi);
            *(LDSP u32x4*)(zw + (((4 * Tp + q) ^ (rho & 7)) << 4)) = o;
            if (rho == 0) {
                if (Tp == 1) {
#pragma unroll
                    for (int i = 0; i < 4; ++i) *(LDSP u32*)(zw + 32 * 128 + ((15 + 4 * q + i) << 2)) = o[i];
                } else if (q == 0) {
                    *(LDSP u32*)(zw + 32 * 128 + 31 * 4) = o[0];
                }
            }
        }
        BAR();
        {
            const LDSP char* zq = sm + L_ZS + q * ZP;
            const u32x4 dv = *(const LDSP u32x4*)(sm + zdo);
            const u32x4 mv = *(const LDSP u32x4*)(sm + zmo);
            f32x4 d[4];
#pragma unroll
            for (int e = 0; e < 4; ++e) d[e] = mma16(u32x2{dv[e], mv[3 - e]}, Sep, z4);
            PIN();
#pragma unroll
            for (int e = 0; e < 4; ++e) {
                g1[e][0] = pk(d[e][0], d[e][1]); g1[e][2] = pk(d[e][2], d[e][3]);
                __builtin_amdgcn_raw_buffer_store_b128(g1[e], rs, vo + e * 256, 0, SC1);
            }
            if (wv == 6) {
                const u32 v = *(const LDSP u32*)(zq + 64);
                const f32x4 d = mma16(Sep, u32x2{v, v}, z4);
                if (n < 2) __builtin_amdgcn_raw_buffer_store_b64(u32x2{n ? pk(d[2], d[3]) : pk(d[0], d[1]), TAG}, rs, xpos + (2 * q + n) * 2048, 0, SC1);
            }
        }
        PIN();
        WLOADS(NW2, 8);
        const __amdgpu_buffer_rsrc_t rg = __builtin_amdgcn_make_buffer_rsrc((void*)gw, 0, 2048, 0x00020000);
        gw0 = __builtin_amdgcn_raw_buffer_load_b128(rg, gch * 16, 0, 0);
        gw1 = __builtin_amdgcn_raw_buffer_load_b128(rg, gch * 16 + 1024, 0, 0);
        PIN();
    }
    if (wv < 6) {
        const bool ua = (wv == 1) || (wv == 2);
        const int be = wv == 0 ? 16 * r : (wv == 1 ? r : 0);
        const int ga = (wv == 1 || wv == 4) ? 8 : ((wv == 3 || wv == 5) ? -128 : 128);
        const int de = wv == 4 ? r : (wv == 5 ? -16 * r : 0);
        const float u = (float)(be + ga * n) * (1.0f / 2048.0f), w = (float)(de * n) * (1.0f / 2048.0f);
        f32x4 cc, ss;
#pragma unroll
        for (int i = 0; i < 4; ++i) {
            const int al = ua ? 8 * (i >> 1) + 2 * q + (i & 1) : 4 * q + i;
            const float a = (float)al * u + w;
            cc[i] = cosr(a); ss[i] = sinr(a);
        }
        u32x4 o0 = __builtin_bit_cast(u32x4, cc), o1 = __builtin_bit_cast(u32x4, ss);
        if (wv != 1 && wv != 4) { o0 = pk4(cc, ss); o1 = pk4(-ss, cc); }
        *(LDSP u32x4*)(sm + L_KC + wv * 2048 + lane * 16) = o0;
        *(LDSP u32x4*)(sm + L_KC + wv * 2048 + 1024 + lane * 16) = o1;
    }
        {
            LDSP char* wt = sm + L_WT + p * WA + (lane & 3) * 2080 + (q * 4 + ((lane >> 2) & 3) + 128 * h) * 4;
#pragma unroll
            for (int e = 0; e < 8; ++e) {
                *(LDSP u32*)(wt + (16 * (e & 3) + 64 * (e >> 2)) * 4) = wl[e][0];
                *(LDSP u32*)(wt + 1024 + (16 * (e & 3) + 64 * (e >> 2)) * 4) = wl[e][1];
            }
            if (c == 31 && (lane & 3) == 3) {
#pragma unroll
                for (int e = 0; e < 8; ++e) *(LDSP u32*)(wt + 2080 + (16 * (e & 3) + 64 * (e >> 2)) * 4) = wl[e][2];
            }
        }
    const int lo2 = (32 * wv + (lane >> 3)) * ROW + (r * 32 + c) * 128;
    int so2[4];
#pragma unroll
    for (int e = 0; e < 4; ++e) { so2[e] = lo2 + (((lane & 7) ^ (lane >> 3)) << 4) + e * 8 * ROW; KEEP(so2[e]); }
    PIN();
    asm volatile("s_waitcnt vmcnt(%0)" :: "n"(8 - NW2 + 2 + DR1) : "memory");
    PIN();
    K2C k;
    f32x4 W[4][2];
    {
        u32x4 a[4];
#pragma unroll
        for (int e = 0; e < 4; ++e) a[e] = __builtin_amdgcn_raw_buffer_load_b128(rs, lo2 + (lane & 7) * 16, e * 8 * ROW, SC1);
        PIN();
        {
            const LDSP char* xs = sm + L_XS + wv * 2048 + lane * 16;
            const u32x4 x0 = *(const LDSP u32x4*)(xs), x1 = *(const LDSP u32x4*)(xs + 1024);
            float acc = 0.f, acc2 = 0.f;
#pragma unroll
            for (int i = 0; i < 4; ++i) { acc = dot2(x0[i], gw0[i], acc); acc2 = dot2(x1[i], gw1[i], acc2); }
            acc += acc2;
            acc = dpp_add<0x111, 0xf>(acc); acc = dpp_add<0x112, 0xf>(acc); acc = dpp_add<0x114, 0xf>(acc); acc = dpp_add<0x118, 0xf>(acc);
            acc = dpp_add<0x142, 0xa>(acc); acc = dpp_add<0x143, 0xc>(acc);
            const float z = bfr(acc);
            const float g = bfr(__builtin_amdgcn_rcpf(1.0f + __expf(-z)));
            const float og = bfr(1.0f - g);
            if (lane == 63) {
                LDSP float* G = (LDSP float*)(sm + L_G) + (wv & 3) * 4 + (wv >> 2);
                G[0] = g; G[2] = og;
            }
        }
        BAR();
        {
            const LDSP char* wt = sm + L_WT + p * 2080 + h * 1024 + lane * 16;
#pragma unroll
            for (int j = 0; j < 4; ++j) {
                const u32x4 v = *(const LDSP u32x4*)(wt + j * WA);
                W[j][0] = f32x4{lo16(v[0]), lo16(v[1]), lo16(v[2]), lo16(v[3])};
                W[j][1] = f32x4{hi16(v[0]), hi16(v[1]), hi16(v[2]), hi16(v[3])};
                KEEP(W[j][0]); KEEP(W[j][1]);
            }
            k2_read(k, sm + L_KC + lane * 16);
        }
        PIN();
        for (unsigned spins = 0;;) {
            u32 bad = 0;
#pragma unroll
            for (int e = 0; e < 4; ++e) bad |= (a[e][1] ^ TAG) | (a[e][3] ^ TAG);
            if (__all(bad == 0) || ++spins > SPIN_CAP) break;
            if (!MASKED) __builtin_amdgcn_s_sleep(2);
            if (!MASKED || bad) {
#pragma unroll
                for (int e = 0; e < 4; ++e) a[e] = __builtin_amdgcn_raw_buffer_load_b128(rs, lo2 + (lane & 7) * 16, e * 8 * ROW, SC1);
            }
        }

        const int qd = (lane >> 1) & 3;
        LDSP char* t1 = sm + L_T1 + qd * 4224 + (qd & 1) * 16 + (qd >> 1) * 64 + h * 512 + p * 128 + (lane & 1) * 32 + (lane >> 3) * 4;
#pragma unroll
        for (int e = 0; e < 4; ++e) {
            *(LDSP u32*)(t1 + (e & 1) * 1024 + (e >> 1) * 64) = a[e][0];
            *(LDSP u32*)(t1 + 2048 + (e & 1) * 1024 + (e >> 1) * 64) = a[e][2];
        }
    }
    BAR();
    {
        const LDSP char* t1 = sm + L_T1 + p * 4224 + (p & 1) * 16 + (p >> 1) * 64 + h * 2048 + lane * 8;
        f32x4 dr[2], di[2];
#pragma unroll
        for (int t = 0; t < 2; ++t) {
            const u32x2 a0 = trd(t1 + t * 1024), a1 = trd(t1 + t * 1024 + 512);
            const u32x4 a = {a0[0], a0[1], a1[0], a1[1]};
            const f32x4 er = mma(a, k.F1r, z4), ei = mma(a, k.F1i, z4);
            const f32x4 c4v = {k.t1c[2 * t], k.t1c[2 * t], k.t1c[2 * t + 1], k.t1c[2 * t + 1]};
            const f32x4 s4v = {k.t1s[2 * t], k.t1s[2 * t], k.t1s[2 * t + 1], k.t1s[2 * t + 1]};
            dr[t] = er * c4v + ei * s4v; di[t] = ei * c4v - er * s4v;
        }
        u32x4 o[2][2];
#pragma unroll
        for (int u = 0; u < 2; ++u) {
            const u32x4 b = {pk(dr[0][u], di[0][u]), pk(dr[0][2 + u], di[0][2 + u]), pk(dr[1][u], di[1][u]), pk(dr[1][2 + u], di[1][2 + u])};
            const f32x4 Xr = mma(k.F2r, b, z4), Xi = mma(k.F2i, b, z4);
            o[0][u] = k2_inv(k, Xr, Xi, W[0][u], W[1][u]);
            o[1][u] = k2_inv(k, Xr, Xi, W[2][u], W[3][u]);
        }
        LDSP char* t2 = sm + L_T2 + (n + 64 * q) * 128 + ((wv ^ (n & 7)) << 4);
#pragma unroll
        for (int i = 0; i < 4; ++i)
            *(LDSP u32x4*)(t2 + i * 2048) = u32x4{o[0][0][i], o[1][0][i], o[0][1][i], o[1][1][i]};
        const LDSP char* t2r = sm + L_T2 + wv * 4096 + lane * 16;
        BAR();
        u32x4 tv[4];
#pragma unroll
        for (int e = 0; e < 4; ++e) tv[e] = *(const LDSP u32x4*)(t2r + e * 1024);
        PIN();
#pragma unroll
        for (int e = 0; e < 4; ++e) __builtin_amdgcn_raw_buffer_store_b128(tv[e], rs, so2[e], 0, SC1);
    }
    u32x4 Fr[2][2], Fi[2][2], Er_[2], Ei_[2], Mix;
    {
        u32 fr[4], fi[4];
#pragma unroll
        for (int i = 0; i < 4; ++i) { fr[i] = pk(c4[i], -s4[i]); fi[i] = pk(s4[i], c4[i]); }
#pragma unroll
        for (int Tp = 0; Tp < 2; ++Tp)
#pragma unroll
            for (int T = 0; T < 2; ++T) {
                const u32 m0 = T ? mn : 0u, m1 = m0 ^ (Tp ? NEG2 : 0u);
                Fr[Tp][T] = u32x4{fr[0] ^ m0, fr[1] ^ m1, fr[2] ^ m0, fr[3] ^ m1};
                Fi[Tp][T] = u32x4{fi[0] ^ m0, fi[1] ^ m1, fi[2] ^ m0, fi[3] ^ m1};
                KEEP(Fr[Tp][T]); KEEP(Fi[Tp][T]);
            }
#pragma unroll
        for (int s = 0; s < 2; ++s) {
            const u32 m0 = s ? mn : 0u, m1 = m0 ^ (h ? NEG2 : 0u);
            Er_[s] = u32x4{fr[0] ^ m0, fr[1] ^ m1, fr[2] ^ m0, fr[3] ^ m1};
            Ei_[s] = u32x4{fi[0] ^ m0, fi[1] ^ m1, fi[2] ^ m0, fi[3] ^ m1};
            KEEP(Er_[s]); KEEP(Ei_[s]);
        }
        const f32x4 G = *(const LDSP f32x4*)(sm + L_G + (n >> 2) * 16);
        const int pp = n >> 2, i = n & 3;
        const float sc = 1.0f / 4194304.0f;
#pragma unroll
        for (int rr = 0; rr < 2; ++rr) {
            const float a = (float)(pp * (2 * q + rr)) * 0.125f;
            const float cc = cosr(a) * sc, ss = sinr(a) * sc;
            const float tau = ((rr == 0) == (i < 2)) ? 1.f : -1.f;
#pragma unroll
            for (int f = 0; f < 2; ++f) {
                const float g0 = G[2 * f], g1 = tau * G[2 * f + 1];
                const float cr = cc * g0 - ss * g1, ci = ss * g0 + cc * g1;
                Mix[2 * rr + f] = (i & 1) ? (i == 1 ? pk(ci, cr) : pk(-ci, -cr)) : pk(cr, -ci);
            }
        }
        KEEP(Mix);
    }
    PIN();
    asm volatile("s_waitcnt vmcnt(%0)" :: "n"(DR2) : "memory");
    PIN();
    {
        const int vo = s1 * ROW + (2 * q) * 4096 + (rho + 128 * p) * 8;
        const int vx = xpos + q * 4096;
        u32x2 a[8], b0 = {0u, 0u}, b1 = {0u, 0u};
        bool ok = false;
        for (unsigned spins = 0;;) {
            if (!MASKED || !ok) {
#pragma unroll
                for (int e = 0; e < 8; ++e) a[e] = __builtin_amdgcn_raw_buffer_load_b64(rs, vo + (e >> 1) * 256, (e & 1) * 4096, SC1);
                if (wv == 6) {
                    b0 = __builtin_amdgcn_raw_buffer_load_b64(rs, vx, 0, SC1);
                    b1 = __builtin_amdgcn_raw_buffer_load_b64(rs, vx + 2048, 0, SC1);
                }
            }
            ok = b0[1] != TAG && b1[1] != TAG;
#pragma unroll
            for (int e = 0; e < 8; ++e) ok = ok & (a[e][1] != TAG);
            if (__all(ok) || ++spins > SPIN_CAP) break;
            if (!MASKED) __builtin_amdgcn_s_sleep(2);
        }
        LDSP char* zq = sm + L_ZS + q * ZP;
        u32x4 dv, mv;
#pragma unroll
        for (int e = 0; e < 4; ++e) {
            f32x4 d = mma(Mix, u32x4{a[2 * e][0], a[2 * e][1], a[2 * e + 1][0], a[2 * e + 1][1]}, z4);
            if (e == 0 && wv == 0 && n == 0) { d[0] = 0.5f * (d[0] + d[2]); d[1] = 0.5f * (d[1] + d[3]); }
            dv[e] = pk(d[0], d[1]);
            mv[3 - e] = pk(d[2], d[3]);
        }
        *(LDSP u32x4*)(sm + zdo) = dv;
        *(LDSP u32x4*)(sm + zmo) = mv;
        if (rho == 0) {
#pragma unroll
            for (int e = 0; e < 4; ++e) {
                const int kk = 32 - 4 * p - e;
                if (kk < 32) *(LDSP u32*)(zq + kk * 4) = mv[3 - e];
            }
        }
        if (wv == 6) {
            const f32x4 d = mma(Mix, u32x4{b0[0], b0[1], b1[0], b1[1]}, z4);
            if (n == 0) *(LDSP u32*)(zq + 64) = pk(0.5f * (d[0] + d[2]), 0.5f * (d[1] + d[3]));
        }
    }
    BAR();
    {
        const LDSP char* zp = sm + L_ZS + p * ZP + n * 128;
        u32x4 Ad[2];
#pragma unroll
        for (int T = 0; T < 2; ++T) {
            const u32x4 a0 = *(const LDSP u32x4*)(zp + T * 2048 + ((q ^ (n & 7)) << 4));
            const u32x4 a1 = *(const LDSP u32x4*)(zp + T * 2048 + (((4 + q) ^ (n & 7)) << 4));
            f32x4 er = mma(a0, Er_[0], z4); er = mma(a1, Er_[1], er);
            f32x4 ei = mma(a0, Ei_[0], z4); ei = mma(a1, Ei_[1], ei);
            Ad[T] = pk4(er * tc[T] - ei * ts[T], ei * tc[T] + er * ts[T]);
        }
        const __amdgpu_buffer_rsrc_t ro = __builtin_amdgcn_make_buffer_rsrc(out, 0, 2048 * 1024 * 2, 0x00020000);
        const int vo = ((s1 + 256 * p) * 1024 + 16 * h + 4 * q + 32 * n) * 2;
#pragma unroll
        for (int Tp = 0; Tp < 2; ++Tp) {
            f32x4 orr = mma(Ad[0], Fr[Tp][0], z4); orr = mma(Ad[1], Fr[Tp][1], orr);
            f32x4 oi = mma(Ad[0], Fi[Tp][0], z4); oi = mma(Ad[1], Fi[Tp][1], oi);
            __builtin_amdgcn_raw_buffer_store_b64(u32x2{pk(orr[0], orr[1]), pk(orr[2], orr[3])}, ro, vo + 1024 * Tp, 0, OUTAUX);
            __builtin_amdgcn_raw_buffer_store_b64(u32x2{pk(oi[0], oi[1]), pk(oi[2], oi[3])}, ro, vo + 1024 * Tp, 2 << 20, OUTAUX);
        }
    }
    asm volatile(".Lgf_tail:");
}

extern "C" void kernel_launch(void* const* d_in, const int* in_sizes, int n_in,
                              void* d_out, int out_size, void* d_ws, size_t ws_size,
                              hipStream_t stream) {
    gf_fused<<<256, 576, 0, stream>>>((const u16*)d_in[0], (const u16*)d_in[6], (const u16*)d_in[1], (const u16*)d_in[2],
                                      (const u16*)d_in[4], (const u16*)d_in[5], (char*)d_ws, (u16*)d_out);
}
```

```cpp
#define NW0 8
#define NW1 8
#define WAUX 2
#include <hip/hip_runtime.h>
#include <hip/hip_bf16.h>
#include <stdint.h>

typedef unsigned short u16;
typedef unsigned int u32;
typedef unsigned long long u64;
typedef __attribute__((ext_vector_type(8))) __bf16 bf16x8;
typedef __attribute__((ext_vector_type(2))) __bf16 bf16x2;
typedef __attribute__((ext_vector_type(4))) short s16x4;
typedef __attribute__((ext_vector_type(4))) float f32x4;
typedef __attribute__((ext_vector_type(2))) float f32x2;
typedef __attribute__((ext_vector_type(4))) unsigned u32x4;
typedef __attribute__((ext_vector_type(2))) unsigned u32x2;
typedef __attribute__((ext_vector_type(3))) unsigned u32x3;
#define LDSP __attribute__((address_space(3)))
#define DI __device__ __forceinline__
#define WARMUP 0
#define EARLYC 1
#define NW2 8
#define DR1 0
#define DR2 0
#define MASKED 1
#define XNT ""
#define OUTAUX 0

DI u32 pk(float lo, float hi) {
    f32x2 t = {lo, hi};
    return __builtin_bit_cast(u32, __builtin_convertvector(t, bf16x2));
}
DI u32x4 pk4(f32x4 a, f32x4 b) { return u32x4{pk(a[0], b[0]), pk(a[1], b[1]), pk(a[2], b[2]), pk(a[3], b[3])}; }
DI float bfr(float v) { return (float)(__bf16)v; }
DI f32x4 mma(u32x4 a, u32x4 b, f32x4 c) {
    return __builtin_amdgcn_mfma_f32_16x16x32_bf16(__builtin_bit_cast(bf16x8, a), __builtin_bit_cast(bf16x8, b), c, 0, 0, 0);
}
DI f32x4 mma16(u32x2 a, u32x2 b, f32x4 c) {
    return __builtin_amdgcn_mfma_f32_16x16x16bf16_1k(__builtin_bit_cast(s16x4, a), __builtin_bit_cast(s16x4, b), c, 0, 0, 0);
}
DI float dot2(u32 a, u32 b, float c) {
    return __builtin_amdgcn_fdot2_f32_bf16(__builtin_bit_cast(bf16x2, a), __builtin_bit_cast(bf16x2, b), c, false);
}
DI float cosr(float x) { return __builtin_amdgcn_cosf(x); }
DI float sinr(float x) { return __builtin_amdgcn_sinf(x); }
DI float lo16(u32 v) { return __builtin_bit_cast(float, v << 16); }
DI float hi16(u32 v) { return __builtin_bit_cast(float, v & 0xffff0000u); }
DI u32x2 trd(const LDSP char* p) {
    return __builtin_bit_cast(u32x2, __builtin_amdgcn_ds_read_tr16_b64_v4i16((LDSP s16x4*)p));
}
template <int CTRL, int RMASK> DI float dpp_add(float v) {
    const int t = __builtin_amdgcn_update_dpp(0, __builtin_bit_cast(int, v), CTRL, RMASK, 0xf, false);
    return v + __builtin_bit_cast(float, t);
}
#define BAR() asm volatile("s_waitcnt lgkmcnt(0)\n\ts_barrier" ::: "memory")
#define PIN() __builtin_amdgcn_sched_barrier(0)
#define KEEP(x) asm volatile("" : "+v"(x))

static constexpr u32 TAG = 0xFFAB7F95u;
static constexpr int SC1 = 16;
static constexpr int ROW = 32768 + 128;
static constexpr int XOFF = 256 * ROW;
static constexpr int WS_BYTES = XOFF + 8 * 2048;
static constexpr int ZP = 33 * 128;
static constexpr int WA = 4 * 2080 + 1024;
static constexpr int L_XS = 0;
static constexpr int L_ZS = 16384;
static constexpr int L_WT = L_ZS + 4 * ZP;
static constexpr int L_T1 = L_WT + 4 * WA;
static constexpr int L_T2 = L_T1 + 4 * 4224;
static constexpr int L_KC = L_T2 + 32768;
static constexpr int L_G = L_KC + 12288;
static constexpr int L_DUM = L_G + 64;
static constexpr int L_END = L_DUM + 256;
static constexpr unsigned SPIN_CAP = 1u << 16;
static constexpr u32 NEG2 = 0x80008000u;

struct K2C {
    u32x4 F1r, F1i, F2r, F2i, G1r, G1i, G2r, G2i;
    f32x4 t1c, t1s, t2c, t2s;
};

DI void k2_read(K2C& k, const LDSP char* kc) {
    k.F1r = *(const LDSP u32x4*)(kc); k.F1i = *(const LDSP u32x4*)(kc + 1024);
    k.t1c = *(const LDSP f32x4*)(kc + 2048); k.t1s = *(const LDSP f32x4*)(kc + 3072);
    k.F2r = *(const LDSP u32x4*)(kc + 4096); k.F2i = *(const LDSP u32x4*)(kc + 5120);
    k.G1r = *(const LDSP u32x4*)(kc + 6144); k.G1i = *(const LDSP u32x4*)(kc + 7168);
    k.t2c = *(const LDSP f32x4*)(kc + 8192); k.t2s = *(const LDSP f32x4*)(kc + 9216);
    k.G2r = *(const LDSP u32x4*)(kc + 10240); k.G2i = *(const LDSP u32x4*)(kc + 11264);
    KEEP(k.F1r); KEEP(k.F1i); KEEP(k.F2r); KEEP(k.F2i); KEEP(k.G1r); KEEP(k.G1i); KEEP(k.G2r); KEEP(k.G2i);
    KEEP(k.t1c); KEEP(k.t1s); KEEP(k.t2c); KEEP(k.t2s);
}
DI u32x4 k2_inv(const K2C& k, f32x4 Xr, f32x4 Xi, f32x4 wr, f32x4 wi) {
    const f32x4 z4 = {0.f, 0.f, 0.f, 0.f};
    const u32x4 y = pk4(Xr * wr - Xi * wi, Xr * wi + Xi * wr);
    const f32x4 er = mma(y, k.G1r, z4), ei = mma(y, k.G1i, z4);
    const u32x4 b = pk4(er * k.t2c - ei * k.t2s, ei * k.t2c + er * k.t2s);
    return pk4(mma(k.G2r, b, z4), mma(k.G2i, b, z4));
}

__global__ __launch_bounds__(576) void gf_fused(const u16* __restrict__ x, const u16* __restrict__ gw,
                                                const u16* __restrict__ w0, const u16* __restrict__ w1,
                                                const u16* __restrict__ w2, const u16* __restrict__ w3,
                                                char* ws, u16* out) {
    __shared__ __attribute__((aligned(16))) char smem[L_END];
    LDSP char* sm = (LDSP char*)smem;
    const int tid = threadIdx.x, lane = tid & 63;
    const int wv = __builtin_amdgcn_readfirstlane(tid >> 6);
    const int p = wv >> 1, h = wv & 1;
    const int q = lane >> 4, n = lane & 15;
    const int id = blockIdx.x;
    const int s1 = (id & 7) * 32 + (id >> 3);
    const int r = id & 7, c = id >> 3;
    const f32x4 z4 = {0.f, 0.f, 0.f, 0.f};
    const __amdgpu_buffer_rsrc_t rs = __builtin_amdgcn_make_buffer_rsrc(ws, 0, WS_BYTES, 0x00020000);

    if (wv == 8) {
        if (c != 31) return;
        u32 last[4];
#pragma unroll
        for (int j = 0; j < 4; ++j) last[j] = *(const u32*)((j == 0 ? w0 : j == 1 ? w1 : j == 2 ? w2 : w3) + 2048 * 513 - 2);
        BAR(); BAR(); BAR();
        const int xo = XOFF + r * 2048 + ((8 * ((n >> 1) & 1) + 2 * (n >> 2) + (n & 1)) + 16 * q) * 32;
        u32x4 xa[2];
#pragma unroll
        for (int e = 0; e < 2; ++e) xa[e] = __builtin_amdgcn_raw_buffer_load_b128(rs, xo + e * 16, 0, SC1);
        PIN();
        K2C k;
        k2_read(k, sm + L_KC + lane * 16);
        f32x4 wf[4];
#pragma unroll
        for (int j = 0; j < 4; ++j) {
            const u32x4 v = *(const LDSP u32x4*)(sm + L_WT + j * WA + 8320 + lane * 16);
            wf[j] = f32x4{lo16(v[0]), lo16(v[1]), lo16(v[2]), lo16(v[3])};
            if (r == 7 && lane == 63) wf[j][3] = hi16(last[j]);
            KEEP(wf[j]);
        }
        BAR();
        for (unsigned spins = 0;;) {
            u32 bad = 0;
#pragma unroll
            for (int e = 0; e < 2; ++e) bad |= (xa[e][1] ^ TAG) | (xa[e][3] ^ TAG);
            if (__all(bad == 0) || ++spins > SPIN_CAP) break;
#pragma unroll
            for (int e = 0; e < 2; ++e) xa[e] = __builtin_amdgcn_raw_buffer_load_b128(rs, xo + e * 16, 0, SC1);
        }
        const u32x4 f = {xa[0][0], xa[0][2], xa[1][0], xa[1][2]};
        const f32x4 er = mma(f, k.F1r, z4), ei = mma(f, k.F1i, z4);
        const u32x4 b = pk4(er * k.t1c + ei * k.t1s, ei * k.t1c - er * k.t1s);
        const f32x4 Xr = mma(k.F2r, b, z4), Xi = mma(k.F2i, b, z4);
        const u32x4 oa = k2_inv(k, Xr, Xi, wf[0], wf[1]), ob = k2_inv(k, Xr, Xi, wf[2], wf[3]);
        const int vs = XOFF + r * 2048 + lane * 32;
        __builtin_amdgcn_raw_buffer_store_b128(u32x4{oa[0], ob[0], oa[1], ob[1]}, rs, vs, 0, SC1);
        __builtin_amdgcn_raw_buffer_store_b128(u32x4{oa[2], ob[2], oa[3], ob[3]}, rs, vs + 16, 0, SC1);
        BAR();
        return;
    }
    const int xpos = XOFF + (((s1 & 15) + 16 * (s1 >> 6)) * 4 + ((s1 >> 4) & 3)) * 8;
    const int rho = 16 * h + n;
    const u32 mn = (n & 1) ? NEG2 : 0u;
    float c4[4], s4[4];
#pragma unroll
    for (int i = 0; i < 4; ++i) {
        const float a = (float)((4 * q + i) * n) * (1.0f / 32.0f);
        c4[i] = cosr(a); s4[i] = sinr(a);
        KEEP(c4[i]); KEEP(s4[i]);
    }
    PIN();
    const int gch = lane ^ (((lane >> 4) & 1) << 1);
    const u16* wp = p == 0 ? w0 : p == 1 ? w1 : p == 2 ? w2 : w3;
    {
        const u16* r0 = x + (size_t)(s1 + 256 * wv) * 1024;
        const u32 vo = gch * 16;
        const u32 l0 = (u32)(size_t)(sm + L_XS) + wv * 2048;
        u32 keep;
        asm volatile("s_mov_b32 %0, m0\n\ts_mov_b32 m0, %3\n\ts_nop 0\n\t"
                     "global_load_lds_dwordx4 %1, %2" XNT "\n\tglobal_load_lds_dwordx4 %1, %2 offset:1024" XNT "\n\t"
                     "s_mov_b32 m0, %0"
                     : "=&s"(keep) : "v"(vo), "s"(r0), "s"(l0) : "memory");
    }
    PIN();

    const __amdgpu_buffer_rsrc_t rw = __builtin_amdgcn_make_buffer_rsrc((void*)wp, 0, 2048 * 513 * 2, 0x00020000);
    const int wvo = ((r + 8 * (q + 16 * ((lane >> 2) & 3) + 128 * h)) * 513 + 16 * c + 4 * (lane & 3)) * 2;
    u32x3 wl[8];
#define WLOADS(lo, hi) do { PIN(); \
        if (c == 31) { _Pragma("unroll") for (int e = lo; e < hi; ++e) \
            wl[e] = __builtin_amdgcn_raw_buffer_load_b96(rw, wvo, (4 * (e & 3) + 64 * (e >> 2)) * 8 * 513 * 2, WAUX); } \
        else { _Pragma("unroll") for (int e = lo; e < hi; ++e) { \
            const u32x2 t_ = __builtin_amdgcn_raw_buffer_load_b64(rw, wvo, (4 * (e & 3) + 64 * (e >> 2)) * 8 * 513 * 2, WAUX); \
            wl[e][0] = t_[0]; wl[e][1] = t_[1]; } } \
        PIN(); } while (0)
    WLOADS(0, NW0);

    f32x4 tc[2], ts[2];
#pragma unroll
    for (int T = 0; T < 2; ++T)
#pragma unroll
        for (int i = 0; i < 4; ++i) {
            const float a = (float)((16 * T + 4 * q + i) * rho) * (1.0f / 1024.0f);
            tc[T][i] = cosr(a); ts[T][i] = sinr(a);
        }
    u32x4 gw0, gw1;
    int zdo = L_ZS + q * ZP + rho * 128 + ((p ^ (rho & 7)) << 4);
    int zmo = L_ZS + q * ZP + (32 - rho) * 128 + (((7 - p) ^ ((32 - rho) & 7)) << 4);
    {
        const u32 c01 = pk(c4[0], c4[1]), c23 = pk(c4[2], c4[3]), s01 = pk(s4[0], s4[1]), s23 = pk(s4[2], s4[3]);
        const u32 mh = h ? 0x80000000u : 0u;
        u32x4 Cf = {c01 ^ mh, c23 ^ mh, c01 ^ mh ^ mn, c23 ^ mh ^ mn};
        u32x4 Sf = {s01 ^ mh, s23 ^ mh, s01 ^ mh ^ mn, s23 ^ mh ^ mn};
        u32x4 Nf = Sf ^ NEG2;
        u32x4 Are[2][2], Aim[2][2];
#pragma unroll
        for (int Tp = 0; Tp < 2; ++Tp)
#pragma unroll
            for (int T = 0; T < 2; ++T) {
                const u32 m = (Tp ? 0x80000000u : 0u) ^ (T ? mn : 0u);
                Are[Tp][T] = u32x4{c01 ^ m, c23 ^ m, s01 ^ m, s23 ^ m};
                Aim[Tp][T] = u32x4{s01 ^ m ^ NEG2, s23 ^ m ^ NEG2, c01 ^ m, c23 ^ m};
                KEEP(Are[Tp][T]); KEEP(Aim[Tp][T]);
            }
        u32x2 Sep;
        {
            const int rr = n >> 1, part = n & 1, sg = (rr & 1) ? -1 : 1;
            const float aa = (float)(q * rr + sg) * 0.125f, ab = (float)(q * rr - sg) * 0.125f;
            const float R2 = 1.41421356237f;
            const float ar = R2 * cosr(aa), ai = -R2 * sinr(aa), br = R2 * cosr(ab), bi = -R2 * sinr(ab);
            Sep[0] = part ? pk(ai, ar) : pk(ar, -ai);
            Sep[1] = part ? pk(bi, -br) : pk(br, bi);
        }
        KEEP(Cf); KEEP(Sf); KEEP(Nf); KEEP(Sep); KEEP(tc[0]); KEEP(tc[1]); KEEP(ts[0]); KEEP(ts[1]);
        const LDSP char* xs = sm + L_XS + p * 2048;
        const LDSP char* xb0 = xs + 64 * (4 * q + (n >> 2)) + 32 * (q & 1) + 8 * (n & 3);
        const LDSP char* xb1 = xs + 64 * (4 * q + (n >> 2)) + 32 * ((q & 1) ^ 1) + 8 * (n & 3);
        LDSP char* zw = sm + L_ZS + p * ZP + rho * 128;
        int vo = s1 * ROW + (n >> 1) * 4096 + (h + 8 * p) * 128 + q * 32 + (n & 1) * 16;
        u32x4 g1[4];
#pragma unroll
        for (int e = 0; e < 4; ++e) { g1[e] = u32x4{0u, TAG, 0u, TAG}; KEEP(g1[e]); }
        KEEP(vo); KEEP(zdo); KEEP(zmo);
        WLOADS(NW0, NW1);
        asm volatile("s_waitcnt vmcnt(%0)\n\ts_barrier" :: "n"(NW1 + WARMUP) : "memory");
        PIN();
        u32x4 Bd[2];
#pragma unroll
        for (int T = 0; T < 2; ++T) {
            const LDSP char* b = T ? xb1 : xb0;
            const u32x2 a0 = trd(b), a1 = trd(b + 1024), a2 = trd(b + 8192), a3 = trd(b + 9216);
            const u32x4 xr = {a0[0], a0[1], a1[0], a1[1]}, xi = {a2[0], a2[1], a3[0], a3[1]};
            f32x4 Dr = mma(xr, Cf, z4); Dr = mma(xi, Sf, Dr);
            f32x4 Di = mma(xr, Nf, z4); Di = mma(xi, Cf, Di);
            const f32x4 re = Dr * tc[T] + Di * ts[T], im = Di * tc[T] - Dr * ts[T];
            Bd[T] = u32x4{pk(re[0], re[1]), pk(re[2], re[3]), pk(im[0], im[1]), pk(im[2], im[3])};
        }
        WLOADS(NW1, NW2);
#pragma unroll
        for (int Tp = 0; Tp < 2; ++Tp) {
            f32x4 Zr = mma(Are[Tp][0], Bd[0], z4); Zr = mma(Are[Tp][1], Bd[1], Zr);
            f32x4 Zi = mma(Aim[Tp][0], Bd[0], z4); Zi = mma(Aim[Tp][1], Bd[1], Zi);
            const u32x4 o = pk4(Zr, Zi);
            *(LDSP u32x4*)(zw + (((4 * Tp + q) ^ (rho & 7)) << 4)) = o;
            if (rho == 0) {
                if (Tp == 1) {
#pragma unroll
                    for (int i = 0; i < 4; ++i) *(LDSP u32*)(zw + 32 * 128 + ((15 + 4 * q + i) << 2)) = o[i];
                } else if (q == 0) {
                    *(LDSP u32*)(zw + 32 * 128 + 31 * 4) = o[0];
                }
            }
        }
        BAR();
        {
            const LDSP char* zq = sm + L_ZS + q * ZP;
            const u32x4 dv = *(const LDSP u32x4*)(sm + zdo);
            const u32x4 mv = *(const LDSP u32x4*)(sm + zmo);
            f32x4 d[4];
#pragma unroll
            for (int e = 0; e < 4; ++e) d[e] = mma16(u32x2{dv[e], mv[3 - e]}, Sep, z4);
            PIN();
#pragma unroll
            for (int e = 0; e < 4; ++e) {
                g1[e][0] = pk(d[e][0], d[e][1]); g1[e][2] = pk(d[e][2], d[e][3]);
                __builtin_amdgcn_raw_buffer_store_b128(g1[e], rs, vo + e * 256, 0, SC1);
            }
            if (wv == 6) {
                const u32 v = *(const LDSP u32*)(zq + 64);
                const f32x4 d = mma16(Sep, u32x2{v, v}, z4);
                if (n < 2) __builtin_amdgcn_raw_buffer_store_b64(u32x2{n ? pk(d[2], d[3]) : pk(d[0], d[1]), TAG}, rs, xpos + (2 * q + n) * 2048, 0, SC1);
            }
        }
        PIN();
        WLOADS(NW2, 8);
        const __amdgpu_buffer_rsrc_t rg = __builtin_amdgcn_make_buffer_rsrc((void*)gw, 0, 2048, 0x00020000);
        gw0 = __builtin_amdgcn_raw_buffer_load_b128(rg, gch * 16, 0, 0);
        gw1 = __builtin_amdgcn_raw_buffer_load_b128(rg, gch * 16 + 1024, 0, 0);
        PIN();
    }
    if (wv < 6) {
        const bool ua = (wv == 1) || (wv == 2);
        const int be = wv == 0 ? 16 * r : (wv == 1 ? r : 0);
        const int ga = (wv == 1 || wv == 4) ? 8 : ((wv == 3 || wv == 5) ? -128 : 128);
        const int de = wv == 4 ? r : (wv == 5 ? -16 * r : 0);
        const float u = (float)(be + ga * n) * (1.0f / 2048.0f), w = (float)(de * n) * (1.0f / 2048.0f);
        f32x4 cc, ss;
#pragma unroll
        for (int i = 0; i < 4; ++i) {
            const int al = ua ? 8 * (i >> 1) + 2 * q + (i & 1) : 4 * q + i;
            const float a = (float)al * u + w;
            cc[i] = cosr(a); ss[i] = sinr(a);
        }
        u32x4 o0 = __builtin_bit_cast(u32x4, cc), o1 = __builtin_bit_cast(u32x4, ss);
        if (wv != 1 && wv != 4) { o0 = pk4(cc, ss); o1 = pk4(-ss, cc); }
        *(LDSP u32x4*)(sm + L_KC + wv * 2048 + lane * 16) = o0;
        *(LDSP u32x4*)(sm + L_KC + wv * 2048 + 1024 + lane * 16) = o1;
    }
        {
            LDSP char* wt = sm + L_WT + p * WA + (lane & 3) * 2080 + (q * 4 + ((lane >> 2) & 3) + 128 * h) * 4;
#pragma unroll
            for (int e = 0; e < 8; ++e) {
                *(LDSP u32*)(wt + (16 * (e & 3) + 64 * (e >> 2)) * 4) = wl[e][0];
                *(LDSP u32*)(wt + 1024 + (16 * (e & 3) + 64 * (e >> 2)) * 4) = wl[e][1];
            }
            if (c == 31 && (lane & 3) == 3) {
#pragma unroll
                for (int e = 0; e < 8; ++e) *(LDSP u32*)(wt + 2080 + (16 * (e & 3) + 64 * (e >> 2)) * 4) = wl[e][2];
            }
        }
    const int lo2 = (32 * wv + (lane >> 3)) * ROW + (r * 32 + c) * 128;
    int so2[4];
#pragma unroll
    for (int e = 0; e < 4; ++e) { so2[e] = lo2 + (((lane & 7) ^ (lane >> 3)) << 4) + e * 8 * ROW; KEEP(so2[e]); }
    PIN();
    asm volatile("s_waitcnt vmcnt(%0)" :: "n"(8 - NW2 + 2 + DR1) : "memory");
    PIN();
    K2C k;
    f32x4 W[4][2];
    {
        u32x4 a[4];
#pragma unroll
        for (int e = 0; e < 4; ++e) a[e] = __builtin_amdgcn_raw_buffer_load_b128(rs, lo2 + (lane & 7) * 16, e * 8 * ROW, SC1);
        PIN();
        {
            const LDSP char* xs = sm + L_XS + wv * 2048 + lane * 16;
            const u32x4 x0 = *(const LDSP u32x4*)(xs), x1 = *(const LDSP u32x4*)(xs + 1024);
            float acc = 0.f, acc2 = 0.f;
#pragma unroll
            for (int i = 0; i < 4; ++i) { acc = dot2(x0[i], gw0[i], acc); acc2 = dot2(x1[i], gw1[i], acc2); }
            acc += acc2;
            acc = dpp_add<0x111, 0xf>(acc); acc = dpp_add<0x112, 0xf>(acc); acc = dpp_add<0x114, 0xf>(acc); acc = dpp_add<0x118, 0xf>(acc);
            acc = dpp_add<0x142, 0xa>(acc); acc = dpp_add<0x143, 0xc>(acc);
            const float z = bfr(acc);
            const float g = bfr(__builtin_amdgcn_rcpf(1.0f + __expf(-z)));
            const float og = bfr(1.0f - g);
            if (lane == 63) {
                LDSP float* G = (LDSP float*)(sm + L_G) + (wv & 3) * 4 + (wv >> 2);
                G[0] = g; G[2] = og;
            }
        }
        BAR();
        {
            const LDSP char* wt = sm + L_WT + p * 2080 + h * 1024 + lane * 16;
#pragma unroll
            for (int j = 0; j < 4; ++j) {
                const u32x4 v = *(const LDSP u32x4*)(wt + j * WA);
                W[j][0] = f32x4{lo16(v[0]), lo16(v[1]), lo16(v[2]), lo16(v[3])};
                W[j][1] = f32x4{hi16(v[0]), hi16(v[1]), hi16(v[2]), hi16(v[3])};
                KEEP(W[j][0]); KEEP(W[j][1]);
            }
            k2_read(k, sm + L_KC + lane * 16);
        }
        PIN();
        for (unsigned spins = 0;;) {
            u32 bad = 0;
#pragma unroll
            for (int e = 0; e < 4; ++e) bad |= (a[e][1] ^ TAG) | (a[e][3] ^ TAG);
            if (__all(bad == 0) || ++spins > SPIN_CAP) break;
            if (!MASKED) __builtin_amdgcn_s_sleep(2);
            if (!MASKED || bad) {
#pragma unroll
                for (int e = 0; e < 4; ++e) a[e] = __builtin_amdgcn_raw_buffer_load_b128(rs, lo2 + (lane & 7) * 16, e * 8 * ROW, SC1);
            }
        }

        const int qd = (lane >> 1) & 3;
        LDSP char* t1 = sm + L_T1 + qd * 4224 + (qd & 1) * 16 + (qd >> 1) * 64 + h * 512 + p * 128 + (lane & 1) * 32 + (lane >> 3) * 4;
#pragma unroll
        for (int e = 0; e < 4; ++e) {
            *(LDSP u32*)(t1 + (e & 1) * 1024 + (e >> 1) * 64) = a[e][0];
            *(LDSP u32*)(t1 + 2048 + (e & 1) * 1024 + (e >> 1) * 64) = a[e][2];
        }
    }
    BAR();
    {
        const LDSP char* t1 = sm + L_T1 + p * 4224 + (p & 1) * 16 + (p >> 1) * 64 + h * 2048 + lane * 8;
        f32x4 dr[2], di[2];
#pragma unroll
        for (int t = 0; t < 2; ++t) {
            const u32x2 a0 = trd(t1 + t * 1024), a1 = trd(t1 + t * 1024 + 512);
            const u32x4 a = {a0[0], a0[1], a1[0], a1[1]};
            const f32x4 er = mma(a, k.F1r, z4), ei = mma(a, k.F1i, z4);
            const f32x4 c4v = {k.t1c[2 * t], k.t1c[2 * t], k.t1c[2 * t + 1], k.t1c[2 * t + 1]};
            const f32x4 s4v = {k.t1s[2 * t], k.t1s[2 * t], k.t1s[2 * t + 1], k.t1s[2 * t + 1]};
            dr[t] = er * c4v + ei * s4v; di[t] = ei * c4v - er * s4v;
        }
        u32x4 o[2][2];
#pragma unroll
        for (int u = 0; u < 2; ++u) {
            const u32x4 b = {pk(dr[0][u], di[0][u]), pk(dr[0][2 + u], di[0][2 + u]), pk(dr[1][u], di[1][u]), pk(dr[1][2 + u], di[1][2 + u])};
            const f32x4 Xr = mma(k.F2r, b, z4), Xi = mma(k.F2i, b, z4);
            o[0][u] = k2_inv(k, Xr, Xi, W[0][u], W[1][u]);
            o[1][u] = k2_inv(k, Xr, Xi, W[2][u], W[3][u]);
        }
        LDSP char* t2 = sm + L_T2 + (n + 64 * q) * 128 + ((wv ^ (n & 7)) << 4);
#pragma unroll
        for (int i = 0; i < 4; ++i)
            *(LDSP u32x4*)(t2 + i * 2048) = u32x4{o[0][0][i], o[1][0][i], o[0][1][i], o[1][1][i]};
        const LDSP char* t2r = sm + L_T2 + wv * 4096 + lane * 16;
        BAR();
        u32x4 tv[4];
#pragma unroll
        for (int e = 0; e < 4; ++e) tv[e] = *(const LDSP u32x4*)(t2r + e * 1024);
        PIN();
#pragma unroll
        for (int e = 0; e < 4; ++e) __builtin_amdgcn_raw_buffer_store_b128(tv[e], rs, so2[e], 0, SC1);
    }
    u32x4 Fr[2][2], Fi[2][2], Er_[2], Ei_[2], Mix;
    {
        u32 fr[4], fi[4];
#pragma unroll
        for (int i = 0; i < 4; ++i) { fr[i] = pk(c4[i], -s4[i]); fi[i] = pk(s4[i], c4[i]); }
#pragma unroll
        for (int Tp = 0; Tp < 2; ++Tp)
#pragma unroll
            for (int T = 0; T < 2; ++T) {
                const u32 m0 = T ? mn : 0u, m1 = m0 ^ (Tp ? NEG2 : 0u);
                Fr[Tp][T] = u32x4{fr[0] ^ m0, fr[1] ^ m1, fr[2] ^ m0, fr[3] ^ m1};
                Fi[Tp][T] = u32x4{fi[0] ^ m0, fi[1] ^ m1, fi[2] ^ m0, fi[3] ^ m1};
                KEEP(Fr[Tp][T]); KEEP(Fi[Tp][T]);
            }
#pragma unroll
        for (int s = 0; s < 2; ++s) {
            const u32 m0 = s ? mn : 0u, m1 = m0 ^ (h ? NEG2 : 0u);
            Er_[s] = u32x4{fr[0] ^ m0, fr[1] ^ m1, fr[2] ^ m0, fr[3] ^ m1};
            Ei_[s] = u32x4{fi[0] ^ m0, fi[1] ^ m1, fi[2] ^ m0, fi[3] ^ m1};
            KEEP(Er_[s]); KEEP(Ei_[s]);
        }
        const f32x4 G = *(const LDSP f32x4*)(sm + L_G + (n >> 2) * 16);
        const int pp = n >> 2, i = n & 3;
        const float sc = 1.0f / 4194304.0f;
#pragma unroll
        for (int rr = 0; rr < 2; ++rr) {
            const float a = (float)(pp * (2 * q + rr)) * 0.125f;
            const float cc = cosr(a) * sc, ss = sinr(a) * sc;
            const float tau = ((rr == 0) == (i < 2)) ? 1.f : -1.f;
#pragma unroll
            for (int f = 0; f < 2; ++f) {
                const float g0 = G[2 * f], g1 = tau * G[2 * f + 1];
                const float cr = cc * g0 - ss * g1, ci = ss * g0 + cc * g1;
                Mix[2 * rr + f] = (i & 1) ? (i == 1 ? pk(ci, cr) : pk(-ci, -cr)) : pk(cr, -ci);
            }
        }
        KEEP(Mix);
    }
    PIN();
    asm volatile("s_waitcnt vmcnt(%0)" :: "n"(DR2) : "memory");
    PIN();
    {
        const int vo = s1 * ROW + (2 * q) * 4096 + (rho + 128 * p) * 8;
        const int vx = xpos + q * 4096;
        u32x2 a[8], b0 = {0u, 0u}, b1 = {0u, 0u};
        bool ok = false;
        for (unsigned spins = 0;;) {
            if (!MASKED || !ok) {
#pragma unroll
                for (int e = 0; e < 8; ++e) a[e] = __builtin_amdgcn_raw_buffer_load_b64(rs, vo + (e >> 1) * 256, (e & 1) * 4096, SC1);
                if (wv == 6) {
                    b0 = __builtin_amdgcn_raw_buffer_load_b64(rs, vx, 0, SC1);
                    b1 = __builtin_amdgcn_raw_buffer_load_b64(rs, vx + 2048, 0, SC1);
                }
            }
            ok = b0[1] != TAG && b1[1] != TAG;
#pragma unroll
            for (int e = 0; e < 8; ++e) ok = ok & (a[e][1] != TAG);
            if (__all(ok) || ++spins > SPIN_CAP) break;
            if (!MASKED) __builtin_amdgcn_s_sleep(2);
        }
        LDSP char* zq = sm + L_ZS + q * ZP;
        u32x4 dv, mv;
#pragma unroll
        for (int e = 0; e < 4; ++e) {
            f32x4 d = mma(Mix, u32x4{a[2 * e][0], a[2 * e][1], a[2 * e + 1][0], a[2 * e + 1][1]}, z4);
            if (e == 0 && wv == 0 && n == 0) { d[0] = 0.5f * (d[0] + d[2]); d[1] = 0.5f * (d[1] + d[3]); }
            dv[e] = pk(d[0], d[1]);
            mv[3 - e] = pk(d[2], d[3]);
        }
        *(LDSP u32x4*)(sm + zdo) = dv;
        *(LDSP u32x4*)(sm + zmo) = mv;
        if (rho == 0) {
#pragma unroll
            for (int e = 0; e < 4; ++e) {
                const int kk = 32 - 4 * p - e;
                if (kk < 32) *(LDSP u32*)(zq + kk * 4) = mv[3 - e];
            }
        }
        if (wv == 6) {
            const f32x4 d = mma(Mix, u32x4{b0[0], b0[1], b1[0], b1[1]}, z4);
            if (n == 0) *(LDSP u32*)(zq + 64) = pk(0.5f * (d[0] + d[2]), 0.5f * (d[1] + d[3]));
        }
    }
    BAR();
    {
        const LDSP char* zp = sm + L_ZS + p * ZP + n * 128;
        u32x4 Ad[2];
#pragma unroll
        for (int T = 0; T < 2; ++T) {
            const u32x4 a0 = *(const LDSP u32x4*)(zp + T * 2048 + ((q ^ (n & 7)) << 4));
            const u32x4 a1 = *(const LDSP u32x4*)(zp + T * 2048 + (((4 + q) ^ (n & 7)) << 4));
            f32x4 er = mma(a0, Er_[0], z4); er = mma(a1, Er_[1], er);
            f32x4 ei = mma(a0, Ei_[0], z4); ei = mma(a1, Ei_[1], ei);
            Ad[T] = pk4(er * tc[T] - ei * ts[T], ei * tc[T] + er * ts[T]);
        }
        const __amdgpu_buffer_rsrc_t ro = __builtin_amdgcn_make_buffer_rsrc(out, 0, 2048 * 1024 * 2, 0x00020000);
        const int vo = ((s1 + 256 * p) * 1024 + 16 * h + 4 * q + 32 * n) * 2;
#pragma unroll
        for (int Tp = 0; Tp < 2; ++Tp) {
            f32x4 orr = mma(Ad[0], Fr[Tp][0], z4); orr = mma(Ad[1], Fr[Tp][1], orr);
            f32x4 oi = mma(Ad[0], Fi[Tp][0], z4); oi = mma(Ad[1], Fi[Tp][1], oi);
            __builtin_amdgcn_raw_buffer_store_b64(u32x2{pk(orr[0], orr[1]), pk(orr[2], orr[3])}, ro, vo + 1024 * Tp, 0, OUTAUX);
            __builtin_amdgcn_raw_buffer_store_b64(u32x2{pk(oi[0], oi[1]), pk(oi[2], oi[3])}, ro, vo + 1024 * Tp, 2 << 20, OUTAUX);
        }
    }
    asm volatile(".Lgf_tail:");
}

extern "C" void kernel_launch(void* const* d_in, const int* in_sizes, int n_in,
                              void* d_out, int out_size, void* d_ws, size_t ws_size,
                              hipStream_t stream) {
    gf_fused<<<256, 576, 0, stream>>>((const u16*)d_in[0], (const u16*)d_in[6], (const u16*)d_in[1], (const u16*)d_in[2],
                                      (const u16*)d_in[4], (const u16*)d_in[5], (char*)d_ws, (u16*)d_out);
}
```

```cpp
#define NW0 0
#define NW1 0
#define NW2 0
#include <hip/hip_runtime.h>
#include <hip/hip_bf16.h>
#include <stdint.h>

typedef unsigned short u16;
typedef unsigned int u32;
typedef unsigned long long u64;
typedef __attribute__((ext_vector_type(8))) __bf16 bf16x8;
typedef __attribute__((ext_vector_type(2))) __bf16 bf16x2;
typedef __attribute__((ext_vector_type(4))) short s16x4;
typedef __attribute__((ext_vector_type(4))) float f32x4;
typedef __attribute__((ext_vector_type(2))) float f32x2;
typedef __attribute__((ext_vector_type(4))) unsigned u32x4;
typedef __attribute__((ext_vector_type(2))) unsigned u32x2;
typedef __attribute__((ext_vector_type(3))) unsigned u32x3;
#define LDSP __attribute__((address_space(3)))
#define DI __device__ __forceinline__

#define RT_(v) do { __builtin_amdgcn_sched_barrier(0); asm volatile("s_memrealtime %0\n\ts_waitcnt lgkmcnt(0)" : "=s"(v) :: "memory"); __builtin_amdgcn_sched_barrier(0);} while (0)
#define WARMUP 0
#define EARLYC 1
#define DR1 0
#define DR2 0
#define MASKED 1
#define WAUX 0
#define XNT ""
#define OUTAUX 0

DI u32 pk(float lo, float hi) {
    f32x2 t = {lo, hi};
    return __builtin_bit_cast(u32, __builtin_convertvector(t, bf16x2));
}
DI u32x4 pk4(f32x4 a, f32x4 b) { return u32x4{pk(a[0], b[0]), pk(a[1], b[1]), pk(a[2], b[2]), pk(a[3], b[3])}; }
DI float bfr(float v) { return (float)(__bf16)v; }
DI f32x4 mma(u32x4 a, u32x4 b, f32x4 c) {
    return __builtin_amdgcn_mfma_f32_16x16x32_bf16(__builtin_bit_cast(bf16x8, a), __builtin_bit_cast(bf16x8, b), c, 0, 0, 0);
}
DI f32x4 mma16(u32x2 a, u32x2 b, f32x4 c) {
    return __builtin_amdgcn_mfma_f32_16x16x16bf16_1k(__builtin_bit_cast(s16x4, a), __builtin_bit_cast(s16x4, b), c, 0, 0, 0);
}
DI float dot2(u32 a, u32 b, float c) {
    return __builtin_amdgcn_fdot2_f32_bf16(__builtin_bit_cast(bf16x2, a), __builtin_bit_cast(bf16x2, b), c, false);
}
DI float cosr(float x) { return __builtin_amdgcn_cosf(x); }
DI float sinr(float x) { return __builtin_amdgcn_sinf(x); }
DI float lo16(u32 v) { return __builtin_bit_cast(float, v << 16); }
DI float hi16(u32 v) { return __builtin_bit_cast(float, v & 0xffff0000u); }
DI u32x2 trd(const LDSP char* p) {
    return __builtin_bit_cast(u32x2, __builtin_amdgcn_ds_read_tr16_b64_v4i16((LDSP s16x4*)p));
}
template <int CTRL, int RMASK> DI float dpp_add(float v) {
    const int t = __builtin_amdgcn_update_dpp(0, __builtin_bit_cast(int, v), CTRL, RMASK, 0xf, false);
    return v + __builtin_bit_cast(float, t);
}
#define BAR() asm volatile("s_waitcnt lgkmcnt(0)\n\ts_barrier" ::: "memory")
#define PIN() __builtin_amdgcn_sched_barrier(0)
#define KEEP(x) asm volatile("" : "+v"(x))

static constexpr u32 TAG = 0xFFAB7F95u;
static constexpr int SC1 = 16;
static constexpr int ROW = 32768 + 128;
static constexpr int XOFF = 256 * ROW;
static constexpr int WS_BYTES = XOFF + 8 * 2048;
static constexpr int ZP = 33 * 128;
static constexpr int WA = 4 * 2080 + 1024;
static constexpr int L_XS = 0;
static constexpr int L_ZS = 16384;
static constexpr int L_WT = L_ZS + 4 * ZP;
static constexpr int L_T1 = L_WT + 4 * WA;
static constexpr int L_T2 = L_T1 + 4 * 4224;
static constexpr int L_KC = L_T2 + 32768;
static constexpr int L_G = L_KC + 12288;
static constexpr int L_DUM = L_G + 64;
static constexpr int L_END = L_DUM + 256;
static constexpr unsigned SPIN_CAP = 1u << 16;
static constexpr u32 NEG2 = 0x80008000u;

struct K2C {
    u32x4 F1r, F1i, F2r, F2i, G1r, G1i, G2r, G2i;
    f32x4 t1c, t1s, t2c, t2s;
};

DI void k2_read(K2C& k, const LDSP char* kc) {
    k.F1r = *(const LDSP u32x4*)(kc); k.F1i = *(const LDSP u32x4*)(kc + 1024);
    k.t1c = *(const LDSP f32x4*)(kc + 2048); k.t1s = *(const LDSP f32x4*)(kc + 3072);
    k.F2r = *(const LDSP u32x4*)(kc + 4096); k.F2i = *(const LDSP u32x4*)(kc + 5120);
    k.G1r = *(const LDSP u32x4*)(kc + 6144); k.G1i = *(const LDSP u32x4*)(kc + 7168);
    k.t2c = *(const LDSP f32x4*)(kc + 8192); k.t2s = *(const LDSP f32x4*)(kc + 9216);
    k.G2r = *(const LDSP u32x4*)(kc + 10240); k.G2i = *(const LDSP u32x4*)(kc + 11264);
    KEEP(k.F1r); KEEP(k.F1i); KEEP(k.F2r); KEEP(k.F2i); KEEP(k.G1r); KEEP(k.G1i); KEEP(k.G2r); KEEP(k.G2i);
    KEEP(k.t1c); KEEP(k.t1s); KEEP(k.t2c); KEEP(k.t2s);
}
DI u32x4 k2_inv(const K2C& k, f32x4 Xr, f32x4 Xi, f32x4 wr, f32x4 wi) {
    const f32x4 z4 = {0.f, 0.f, 0.f, 0.f};
    const u32x4 y = pk4(Xr * wr - Xi * wi, Xr * wi + Xi * wr);
    const f32x4 er = mma(y, k.G1r, z4), ei = mma(y, k.G1i, z4);
    const u32x4 b = pk4(er * k.t2c - ei * k.t2s, ei * k.t2c + er * k.t2s);
    return pk4(mma(k.G2r, b, z4), mma(k.G2i, b, z4));
}

__global__ __launch_bounds__(576) void gf_fused(const u16* __restrict__ x, const u16* __restrict__ gw,
                                                const u16* __restrict__ w0, const u16* __restrict__ w1,
                                                const u16* __restrict__ w2, const u16* __restrict__ w3,
                                                char* ws, u16* out) {
    __shared__ __attribute__((aligned(16))) char smem[L_END];
    LDSP char* sm = (LDSP char*)smem;
    unsigned long long dt0_, dtj_ = 0; RT_(dt0_);
    const int tid = threadIdx.x, lane = tid & 63;
    const int wv = __builtin_amdgcn_readfirstlane(tid >> 6);
    const int p = wv >> 1, h = wv & 1;
    const int q = lane >> 4, n = lane & 15;
    const int id = blockIdx.x;
    const int s1 = (id & 7) * 32 + (id >> 3);
    const int r = id & 7, c = id >> 3;
    const f32x4 z4 = {0.f, 0.f, 0.f, 0.f};
    const __amdgpu_buffer_rsrc_t rs = __builtin_amdgcn_make_buffer_rsrc(ws, 0, WS_BYTES, 0x00020000);

    if (wv == 8) {
        if (c != 31) return;
        u32 last[4];
#pragma unroll
        for (int j = 0; j < 4; ++j) last[j] = *(const u32*)((j == 0 ? w0 : j == 1 ? w1 : j == 2 ? w2 : w3) + 2048 * 513 - 2);
        BAR(); BAR(); BAR();
        const int xo = XOFF + r * 2048 + ((8 * ((n >> 1) & 1) + 2 * (n >> 2) + (n & 1)) + 16 * q) * 32;
        u32x4 xa[2];
#pragma unroll
        for (int e = 0; e < 2; ++e) xa[e] = __builtin_amdgcn_raw_buffer_load_b128(rs, xo + e * 16, 0, SC1);
        PIN();
        K2C k;
        k2_read(k, sm + L_KC + lane * 16);
        f32x4 wf[4];
#pragma unroll
        for (int j = 0; j < 4; ++j) {
            const u32x4 v = *(const LDSP u32x4*)(sm + L_WT + j * WA + 8320 + lane * 16);
            wf[j] = f32x4{lo16(v[0]), lo16(v[1]), lo16(v[2]), lo16(v[3])};
            if (r == 7 && lane == 63) wf[j][3] = hi16(last[j]);
            KEEP(wf[j]);
        }
        BAR();
        for (unsigned spins = 0;;) {
            u32 bad = 0;
#pragma unroll
            for (int e = 0; e < 2; ++e) bad |= (xa[e][1] ^ TAG) | (xa[e][3] ^ TAG);
            if (__all(bad == 0) || ++spins > SPIN_CAP) break;
#pragma unroll
            for (int e = 0; e < 2; ++e) xa[e] = __builtin_amdgcn_raw_buffer_load_b128(rs, xo + e * 16, 0, SC1);
        }
        const u32x4 f = {xa[0][0], xa[0][2], xa[1][0], xa[1][2]};
        const f32x4 er = mma(f, k.F1r, z4), ei = mma(f, k.F1i, z4);
        const u32x4 b = pk4(er * k.t1c + ei * k.t1s, ei * k.t1c - er * k.t1s);
        const f32x4 Xr = mma(k.F2r, b, z4), Xi = mma(k.F2i, b, z4);
        const u32x4 oa = k2_inv(k, Xr, Xi, wf[0], wf[1]), ob = k2_inv(k, Xr, Xi, wf[2], wf[3]);
        const int vs = XOFF + r * 2048 + lane * 32;
        __builtin_amdgcn_raw_buffer_store_b128(u32x4{oa[0], ob[0], oa[1], ob[1]}, rs, vs, 0, SC1);
        __builtin_amdgcn_raw_buffer_store_b128(u32x4{oa[2], ob[2], oa[3], ob[3]}, rs, vs + 16, 0, SC1);
        BAR();
        return;
    }
    const int xpos = XOFF + (((s1 & 15) + 16 * (s1 >> 6)) * 4 + ((s1 >> 4) & 3)) * 8;
    const int rho = 16 * h + n;
    const u32 mn = (n & 1) ? NEG2 : 0u;
    float c4[4], s4[4];
#pragma unroll
    for (int i = 0; i < 4; ++i) {
        const float a = (float)((4 * q + i) * n) * (1.0f / 32.0f);
        c4[i] = cosr(a); s4[i] = sinr(a);
        KEEP(c4[i]); KEEP(s4[i]);
    }
    PIN();
    const int gch = lane ^ (((lane >> 4) & 1) << 1);
    const u16* wp = p == 0 ? w0 : p == 1 ? w1 : p == 2 ? w2 : w3;
    {
        const u16* r0 = x + (size_t)(s1 + 256 * wv) * 1024;
        const u32 vo = gch * 16;
        const u32 l0 = (u32)(size_t)(sm + L_XS) + wv * 2048;
        u32 keep;
        asm volatile("s_mov_b32 %0, m0\n\ts_mov_b32 m0, %3\n\ts_nop 0\n\t"
                     "global_load_lds_dwordx4 %1, %2" XNT "\n\tglobal_load_lds_dwordx4 %1, %2 offset:1024" XNT "\n\t"
                     "s_mov_b32 m0, %0"
                     : "=&s"(keep) : "v"(vo), "s"(r0), "s"(l0) : "memory");
    }
    PIN();

    const __amdgpu_buffer_rsrc_t rw = __builtin_amdgcn_make_buffer_rsrc((void*)wp, 0, 2048 * 513 * 2, 0x00020000);
    const int wvo = ((r + 8 * (q + 16 * ((lane >> 2) & 3) + 128 * h)) * 513 + 16 * c + 4 * (lane & 3)) * 2;
    u32x3 wl[8];
#define WLOADS(lo, hi) do { PIN(); \
        if (c == 31) { _Pragma("unroll") for (int e = lo; e < hi; ++e) \
            wl[e] = __builtin_amdgcn_raw_buffer_load_b96(rw, wvo, (4 * (e & 3) + 64 * (e >> 2)) * 8 * 513 * 2, WAUX); } \
        else { _Pragma("unroll") for (int e = lo; e < hi; ++e) { \
            const u32x2 t_ = __builtin_amdgcn_raw_buffer_load_b64(rw, wvo, (4 * (e & 3) + 64 * (e >> 2)) * 8 * 513 * 2, WAUX); \
            wl[e][0] = t_[0]; wl[e][1] = t_[1]; } } \
        PIN(); } while (0)
    WLOADS(0, NW0);

    f32x4 tc[2], ts[2];
#pragma unroll
    for (int T = 0; T < 2; ++T)
#pragma unroll
        for (int i = 0; i < 4; ++i) {
            const float a = (float)((16 * T + 4 * q + i) * rho) * (1.0f / 1024.0f);
            tc[T][i] = cosr(a); ts[T][i] = sinr(a);
        }
    u32x4 gw0, gw1;
    int zdo = L_ZS + q * ZP + rho * 128 + ((p ^ (rho & 7)) << 4);
    int zmo = L_ZS + q * ZP + (32 - rho) * 128 + (((7 - p) ^ ((32 - rho) & 7)) << 4);
    {
        const u32 c01 = pk(c4[0], c4[1]), c23 = pk(c4[2], c4[3]), s01 = pk(s4[0], s4[1]), s23 = pk(s4[2], s4[3]);
        const u32 mh = h ? 0x80000000u : 0u;
        u32x4 Cf = {c01 ^ mh, c23 ^ mh, c01 ^ mh ^ mn, c23 ^ mh ^ mn};
        u32x4 Sf = {s01 ^ mh, s23 ^ mh, s01 ^ mh ^ mn, s23 ^ mh ^ mn};
        u32x4 Nf = Sf ^ NEG2;
        u32x4 Are[2][2], Aim[2][2];
#pragma unroll
        for (int Tp = 0; Tp < 2; ++Tp)
#pragma unroll
            for (int T = 0; T < 2; ++T) {
                const u32 m = (Tp ? 0x80000000u : 0u) ^ (T ? mn : 0u);
                Are[Tp][T] = u32x4{c01 ^ m, c23 ^ m, s01 ^ m, s23 ^ m};
                Aim[Tp][T] = u32x4{s01 ^ m ^ NEG2, s23 ^ m ^ NEG2, c01 ^ m, c23 ^ m};
                KEEP(Are[Tp][T]); KEEP(Aim[Tp][T]);
            }
        u32x2 Sep;
        {
            const int rr = n >> 1, part = n & 1, sg = (rr & 1) ? -1 : 1;
            const float aa = (float)(q * rr + sg) * 0.125f, ab = (float)(q * rr - sg) * 0.125f;
            const float R2 = 1.41421356237f;
            const float ar = R2 * cosr(aa), ai = -R2 * sinr(aa), br = R2 * cosr(ab), bi = -R2 * sinr(ab);
            Sep[0] = part ? pk(ai, ar) : pk(ar, -ai);
            Sep[1] = part ? pk(bi, -br) : pk(br, bi);
        }
        KEEP(Cf); KEEP(Sf); KEEP(Nf); KEEP(Sep); KEEP(tc[0]); KEEP(tc[1]); KEEP(ts[0]); KEEP(ts[1]);
        const LDSP char* xs = sm + L_XS + p * 2048;
        const LDSP char* xb0 = xs + 64 * (4 * q + (n >> 2)) + 32 * (q & 1) + 8 * (n & 3);
        const LDSP char* xb1 = xs + 64 * (4 * q + (n >> 2)) + 32 * ((q & 1) ^ 1) + 8 * (n & 3);
        LDSP char* zw = sm + L_ZS + p * ZP + rho * 128;
        int vo = s1 * ROW + (n >> 1) * 4096 + (h + 8 * p) * 128 + q * 32 + (n & 1) * 16;
        u32x4 g1[4];
#pragma unroll
        for (int e = 0; e < 4; ++e) { g1[e] = u32x4{0u, TAG, 0u, TAG}; KEEP(g1[e]); }
        KEEP(vo); KEEP(zdo); KEEP(zmo);
        WLOADS(NW0, NW1);
        asm volatile("s_waitcnt vmcnt(%0)\n\ts_barrier" :: "n"(NW1 + WARMUP) : "memory");
        PIN();
        u32x4 Bd[2];
#pragma unroll
        for (int T = 0; T < 2; ++T) {
            const LDSP char* b = T ? xb1 : xb0;
            const u32x2 a0 = trd(b), a1 = trd(b + 1024), a2 = trd(b + 8192), a3 = trd(b + 9216);
            const u32x4 xr = {a0[0], a0[1], a1[0], a1[1]}, xi = {a2[0], a2[1], a3[0], a3[1]};
            f32x4 Dr = mma(xr, Cf, z4); Dr = mma(xi, Sf, Dr);
            f32x4 Di = mma(xr, Nf, z4); Di = mma(xi, Cf, Di);
            const f32x4 re = Dr * tc[T] + Di * ts[T], im = Di * tc[T] - Dr * ts[T];
            Bd[T] = u32x4{pk(re[0], re[1]), pk(re[2], re[3]), pk(im[0], im[1]), pk(im[2], im[3])};
        }
        WLOADS(NW1, NW2);
#pragma unroll
        for (int Tp = 0; Tp < 2; ++Tp) {
            f32x4 Zr = mma(Are[Tp][0], Bd[0], z4); Zr = mma(Are[Tp][1], Bd[1], Zr);
            f32x4 Zi = mma(Aim[Tp][0], Bd[0], z4); Zi = mma(Aim[Tp][1], Bd[1], Zi);
            const u32x4 o = pk4(Zr, Zi);
            *(LDSP u32x4*)(zw + (((4 * Tp + q) ^ (rho & 7)) << 4)) = o;
            if (rho == 0) {
                if (Tp == 1) {
#pragma unroll
                    for (int i = 0; i < 4; ++i) *(LDSP u32*)(zw + 32 * 128 + ((15 + 4 * q + i) << 2)) = o[i];
                } else if (q == 0) {
                    *(LDSP u32*)(zw + 32 * 128 + 31 * 4) = o[0];
                }
            }
        }
        BAR();
        {
            const LDSP char* zq = sm + L_ZS + q * ZP;
            const u32x4 dv = *(const LDSP u32x4*)(sm + zdo);
            const u32x4 mv = *(const LDSP u32x4*)(sm + zmo);
            f32x4 d[4];
#pragma unroll
            for (int e = 0; e < 4; ++e) d[e] = mma16(u32x2{dv[e], mv[3 - e]}, Sep, z4);
            PIN();
#pragma unroll
            for (int e = 0; e < 4; ++e) {
                g1[e][0] = pk(d[e][0], d[e][1]); g1[e][2] = pk(d[e][2], d[e][3]);
                __builtin_amdgcn_raw_buffer_store_b128(g1[e], rs, vo + e * 256, 0, SC1);
            }
            if (wv == 6) {
                const u32 v = *(const LDSP u32*)(zq + 64);
                const f32x4 d = mma16(Sep, u32x2{v, v}, z4);
                if (n < 2) __builtin_amdgcn_raw_buffer_store_b64(u32x2{n ? pk(d[2], d[3]) : pk(d[0], d[1]), TAG}, rs, xpos + (2 * q + n) * 2048, 0, SC1);
            }
        }
        PIN();
        WLOADS(NW2, 8);
        const __amdgpu_buffer_rsrc_t rg = __builtin_amdgcn_make_buffer_rsrc((void*)gw, 0, 2048, 0x00020000);
        gw0 = __builtin_amdgcn_raw_buffer_load_b128(rg, gch * 16, 0, 0);
        gw1 = __builtin_amdgcn_raw_buffer_load_b128(rg, gch * 16 + 1024, 0, 0);
        PIN();
    }
    if (wv < 6) {
        const bool ua = (wv == 1) || (wv == 2);
        const int be = wv == 0 ? 16 * r : (wv == 1 ? r : 0);
        const int ga = (wv == 1 || wv == 4) ? 8 : ((wv == 3 || wv == 5) ? -128 : 128);
        const int de = wv == 4 ? r : (wv == 5 ? -16 * r : 0);
        const float u = (float)(be + ga * n) * (1.0f / 2048.0f), w = (float)(de * n) * (1.0f / 2048.0f);
        f32x4 cc, ss;
#pragma unroll
        for (int i = 0; i < 4; ++i) {
            const int al = ua ? 8 * (i >> 1) + 2 * q + (i & 1) : 4 * q + i;
            const float a = (float)al * u + w;
            cc[i] = cosr(a); ss[i] = sinr(a);
        }
        u32x4 o0 = __builtin_bit_cast(u32x4, cc), o1 = __builtin_bit_cast(u32x4, ss);
        if (wv != 1 && wv != 4) { o0 = pk4(cc, ss); o1 = pk4(-ss, cc); }
        *(LDSP u32x4*)(sm + L_KC + wv * 2048 + lane * 16) = o0;
        *(LDSP u32x4*)(sm + L_KC + wv * 2048 + 1024 + lane * 16) = o1;
    }
    const int lo2 = (32 * wv + (lane >> 3)) * ROW + (r * 32 + c) * 128;
    int so2[4];
#pragma unroll
    for (int e = 0; e < 4; ++e) { so2[e] = lo2 + (((lane & 7) ^ (lane >> 3)) << 4) + e * 8 * ROW; KEEP(so2[e]); }
    PIN();
    asm volatile("s_waitcnt vmcnt(%0)" :: "n"(8 - NW2 + 2 + DR1) : "memory");
    PIN();
    K2C k;
    f32x4 W[4][2];
    {
        u32x4 a[4];
#pragma unroll
        for (int e = 0; e < 4; ++e) a[e] = __builtin_amdgcn_raw_buffer_load_b128(rs, lo2 + (lane & 7) * 16, e * 8 * ROW, SC1);
        PIN();
        {
            LDSP char* wt = sm + L_WT + p * WA + (lane & 3) * 2080 + (q * 4 + ((lane >> 2) & 3) + 128 * h) * 4;
#pragma unroll
            for (int e = 0; e < 8; ++e) {
                *(LDSP u32*)(wt + (16 * (e & 3) + 64 * (e >> 2)) * 4) = wl[e][0];
                *(LDSP u32*)(wt + 1024 + (16 * (e & 3) + 64 * (e >> 2)) * 4) = wl[e][1];
            }
            if (c == 31 && (lane & 3) == 3) {
#pragma unroll
                for (int e = 0; e < 8; ++e) *(LDSP u32*)(wt + 2080 + (16 * (e & 3) + 64 * (e >> 2)) * 4) = wl[e][2];
            }
        }
        {
            const LDSP char* xs = sm + L_XS + wv * 2048 + lane * 16;
            const u32x4 x0 = *(const LDSP u32x4*)(xs), x1 = *(const LDSP u32x4*)(xs + 1024);
            float acc = 0.f, acc2 = 0.f;
#pragma unroll
            for (int i = 0; i < 4; ++i) { acc = dot2(x0[i], gw0[i], acc); acc2 = dot2(x1[i], gw1[i], acc2); }
            acc += acc2;
            acc = dpp_add<0x111, 0xf>(acc); acc = dpp_add<0x112, 0xf>(acc); acc = dpp_add<0x114, 0xf>(acc); acc = dpp_add<0x118, 0xf>(acc);
            acc = dpp_add<0x142, 0xa>(acc); acc = dpp_add<0x143, 0xc>(acc);
            const float z = bfr(acc);
            const float g = bfr(__builtin_amdgcn_rcpf(1.0f + __expf(-z)));
            const float og = bfr(1.0f - g);
            if (lane == 63) {
                LDSP float* G = (LDSP float*)(sm + L_G) + (wv & 3) * 4 + (wv >> 2);
                G[0] = g; G[2] = og;
            }
        }
        BAR();
        {
            const LDSP char* wt = sm + L_WT + p * 2080 + h * 1024 + lane * 16;
#pragma unroll
            for (int j = 0; j < 4; ++j) {
                const u32x4 v = *(const LDSP u32x4*)(wt + j * WA);
                W[j][0] = f32x4{lo16(v[0]), lo16(v[1]), lo16(v[2]), lo16(v[3])};
                W[j][1] = f32x4{hi16(v[0]), hi16(v[1]), hi16(v[2]), hi16(v[3])};
                KEEP(W[j][0]); KEEP(W[j][1]);
            }
            k2_read(k, sm + L_KC + lane * 16);
        }
        PIN();
        for (unsigned spins = 0;;) {
            u32 bad = 0;
#pragma unroll
            for (int e = 0; e < 4; ++e) bad |= (a[e][1] ^ TAG) | (a[e][3] ^ TAG);
            if (__all(bad == 0) || ++spins > SPIN_CAP) break;
            if (!MASKED) __builtin_amdgcn_s_sleep(2);
            if (!MASKED || bad) {
#pragma unroll
                for (int e = 0; e < 4; ++e) a[e] = __builtin_amdgcn_raw_buffer_load_b128(rs, lo2 + (lane & 7) * 16, e * 8 * ROW, SC1);
            }
        }

        const int qd = (lane >> 1) & 3;
        LDSP char* t1 = sm + L_T1 + qd * 4224 + (qd & 1) * 16 + (qd >> 1) * 64 + h * 512 + p * 128 + (lane & 1) * 32 + (lane >> 3) * 4;
#pragma unroll
        for (int e = 0; e < 4; ++e) {
            *(LDSP u32*)(t1 + (e & 1) * 1024 + (e >> 1) * 64) = a[e][0];
            *(LDSP u32*)(t1 + 2048 + (e & 1) * 1024 + (e >> 1) * 64) = a[e][2];
        }
    }
    BAR();
    {
        const LDSP char* t1 = sm + L_T1 + p * 4224 + (p & 1) * 16 + (p >> 1) * 64 + h * 2048 + lane * 8;
        f32x4 dr[2], di[2];
#pragma unroll
        for (int t = 0; t < 2; ++t) {
            const u32x2 a0 = trd(t1 + t * 1024), a1 = trd(t1 + t * 1024 + 512);
            const u32x4 a = {a0[0], a0[1], a1[0], a1[1]};
            const f32x4 er = mma(a, k.F1r, z4), ei = mma(a, k.F1i, z4);
            const f32x4 c4v = {k.t1c[2 * t], k.t1c[2 * t], k.t1c[2 * t + 1], k.t1c[2 * t + 1]};
            const f32x4 s4v = {k.t1s[2 * t], k.t1s[2 * t], k.t1s[2 * t + 1], k.t1s[2 * t + 1]};
            dr[t] = er * c4v + ei * s4v; di[t] = ei * c4v - er * s4v;
        }
        u32x4 o[2][2];
#pragma unroll
        for (int u = 0; u < 2; ++u) {
            const u32x4 b = {pk(dr[0][u], di[0][u]), pk(dr[0][2 + u], di[0][2 + u]), pk(dr[1][u], di[1][u]), pk(dr[1][2 + u], di[1][2 + u])};
            const f32x4 Xr = mma(k.F2r, b, z4), Xi = mma(k.F2i, b, z4);
            o[0][u] = k2_inv(k, Xr, Xi, W[0][u], W[1][u]);
            o[1][u] = k2_inv(k, Xr, Xi, W[2][u], W[3][u]);
        }
        LDSP char* t2 = sm + L_T2 + (n + 64 * q) * 128 + ((wv ^ (n & 7)) << 4);
#pragma unroll
        for (int i = 0; i < 4; ++i)
            *(LDSP u32x4*)(t2 + i * 2048) = u32x4{o[0][0][i], o[1][0][i], o[0][1][i], o[1][1][i]};
        const LDSP char* t2r = sm + L_T2 + wv * 4096 + lane * 16;
        BAR();
        u32x4 tv[4];
#pragma unroll
        for (int e = 0; e < 4; ++e) tv[e] = *(const LDSP u32x4*)(t2r + e * 1024);
        PIN();
#pragma unroll
        for (int e = 0; e < 4; ++e) __builtin_amdgcn_raw_buffer_store_b128(tv[e], rs, so2[e], 0, SC1);
    }
    u32x4 Fr[2][2], Fi[2][2], Er_[2], Ei_[2], Mix;
    {
        u32 fr[4], fi[4];
#pragma unroll
        for (int i = 0; i < 4; ++i) { fr[i] = pk(c4[i], -s4[i]); fi[i] = pk(s4[i], c4[i]); }
#pragma unroll
        for (int Tp = 0; Tp < 2; ++Tp)
#pragma unroll
            for (int T = 0; T < 2; ++T) {
                const u32 m0 = T ? mn : 0u, m1 = m0 ^ (Tp ? NEG2 : 0u);
                Fr[Tp][T] = u32x4{fr[0] ^ m0, fr[1] ^ m1, fr[2] ^ m0, fr[3] ^ m1};
                Fi[Tp][T] = u32x4{fi[0] ^ m0, fi[1] ^ m1, fi[2] ^ m0, fi[3] ^ m1};
                KEEP(Fr[Tp][T]); KEEP(Fi[Tp][T]);
            }
#pragma unroll
        for (int s = 0; s < 2; ++s) {
            const u32 m0 = s ? mn : 0u, m1 = m0 ^ (h ? NEG2 : 0u);
            Er_[s] = u32x4{fr[0] ^ m0, fr[1] ^ m1, fr[2] ^ m0, fr[3] ^ m1};
            Ei_[s] = u32x4{fi[0] ^ m0, fi[1] ^ m1, fi[2] ^ m0, fi[3] ^ m1};
            KEEP(Er_[s]); KEEP(Ei_[s]);
        }
        const f32x4 G = *(const LDSP f32x4*)(sm + L_G + (n >> 2) * 16);
        const int pp = n >> 2, i = n & 3;
        const float sc = 1.0f / 4194304.0f;
#pragma unroll
        for (int rr = 0; rr < 2; ++rr) {
            const float a = (float)(pp * (2 * q + rr)) * 0.125f;
            const float cc = cosr(a) * sc, ss = sinr(a) * sc;
            const float tau = ((rr == 0) == (i < 2)) ? 1.f : -1.f;
#pragma unroll
            for (int f = 0; f < 2; ++f) {
                const float g0 = G[2 * f], g1 = tau * G[2 * f + 1];
                const float cr = cc * g0 - ss * g1, ci = ss * g0 + cc * g1;
                Mix[2 * rr + f] = (i & 1) ? (i == 1 ? pk(ci, cr) : pk(-ci, -cr)) : pk(cr, -ci);
            }
        }
        KEEP(Mix);
    }
    PIN();
    asm volatile("s_waitcnt vmcnt(%0)" :: "n"(DR2) : "memory");
    PIN();
    {
        const int vo = s1 * ROW + (2 * q) * 4096 + (rho + 128 * p) * 8;
        const int vx = xpos + q * 4096;
        u32x2 a[8], b0 = {0u, 0u}, b1 = {0u, 0u};
        bool ok = false;
        for (unsigned spins = 0;;) {
            if (!MASKED || !ok) {
#pragma unroll
                for (int e = 0; e < 8; ++e) a[e] = __builtin_amdgcn_raw_buffer_load_b64(rs, vo + (e >> 1) * 256, (e & 1) * 4096, SC1);
                if (wv == 6) {
                    b0 = __builtin_amdgcn_raw_buffer_load_b64(rs, vx, 0, SC1);
                    b1 = __builtin_amdgcn_raw_buffer_load_b64(rs, vx + 2048, 0, SC1);
                }
            }
            ok = b0[1] != TAG && b1[1] != TAG;
#pragma unroll
            for (int e = 0; e < 8; ++e) ok = ok & (a[e][1] != TAG);
            if (__all(ok) || ++spins > SPIN_CAP) break;
            if (!MASKED) __builtin_amdgcn_s_sleep(2);
        }
        LDSP char* zq = sm + L_ZS + q * ZP;
        u32x4 dv, mv;
#pragma unroll
        for (int e = 0; e < 4; ++e) {
            f32x4 d = mma(Mix, u32x4{a[2 * e][0], a[2 * e][1], a[2 * e + 1][0], a[2 * e + 1][1]}, z4);
            if (e == 0 && wv == 0 && n == 0) { d[0] = 0.5f * (d[0] + d[2]); d[1] = 0.5f * (d[1] + d[3]); }
            dv[e] = pk(d[0], d[1]);
            mv[3 - e] = pk(d[2], d[3]);
        }
        *(LDSP u32x4*)(sm + zdo) = dv;
        *(LDSP u32x4*)(sm + zmo) = mv;
        if (rho == 0) {
#pragma unroll
            for (int e = 0; e < 4; ++e) {
                const int kk = 32 - 4 * p - e;
                if (kk < 32) *(LDSP u32*)(zq + kk * 4) = mv[3 - e];
            }
        }
        if (wv == 6) {
            const f32x4 d = mma(Mix, u32x4{b0[0], b0[1], b1[0], b1[1]}, z4);
            if (n == 0) *(LDSP u32*)(zq + 64) = pk(0.5f * (d[0] + d[2]), 0.5f * (d[1] + d[3]));
        }
    }
    BAR();
    {
        const LDSP char* zp = sm + L_ZS + p * ZP + n * 128;
        u32x4 Ad[2];
#pragma unroll
        for (int T = 0; T < 2; ++T) {
            const u32x4 a0 = *(const LDSP u32x4*)(zp + T * 2048 + ((q ^ (n & 7)) << 4));
            const u32x4 a1 = *(const LDSP u32x4*)(zp + T * 2048 + (((4 + q) ^ (n & 7)) << 4));
            f32x4 er = mma(a0, Er_[0], z4); er = mma(a1, Er_[1], er);
            f32x4 ei = mma(a0, Ei_[0], z4); ei = mma(a1, Ei_[1], ei);
            Ad[T] = pk4(er * tc[T] - ei * ts[T], ei * tc[T] + er * ts[T]);
        }
        const __amdgpu_buffer_rsrc_t ro = __builtin_amdgcn_make_buffer_rsrc(out, 0, 2048 * 1024 * 2, 0x00020000);
        const int vo = ((s1 + 256 * p) * 1024 + 16 * h + 4 * q + 32 * n) * 2;
#pragma unroll
        for (int Tp = 0; Tp < 2; ++Tp) {
            f32x4 orr = mma(Ad[0], Fr[Tp][0], z4); orr = mma(Ad[1], Fr[Tp][1], orr);
            f32x4 oi = mma(Ad[0], Fi[Tp][0], z4); oi = mma(Ad[1], Fi[Tp][1], oi);
            __builtin_amdgcn_raw_buffer_store_b64(u32x2{pk(orr[0], orr[1]), pk(orr[2], orr[3])}, ro, vo + 1024 * Tp, 0, OUTAUX);
            __builtin_amdgcn_raw_buffer_store_b64(u32x2{pk(oi[0], oi[1]), pk(oi[2], oi[3])}, ro, vo + 1024 * Tp, 2 << 20, OUTAUX);
        }
    }
    asm volatile(".Lgf_tail:");
    asm volatile("s_waitcnt vmcnt(0)" ::: "memory");
    dtj_ = dt0_;
    { unsigned long long te_, tn_; RT_(te_); for (int i_ = 0; i_ < 100000; ++i_) { RT_(tn_); if (tn_ - te_ >= dtj_ - dt0_) break; __builtin_amdgcn_s_sleep(1); } }
}

extern "C" void kernel_launch(void* const* d_in, const int* in_sizes, int n_in,
                              void* d_out, int out_size, void* d_ws, size_t ws_size,
                              hipStream_t stream) {
    gf_fused<<<256, 576, 0, stream>>>((const u16*)d_in[0], (const u16*)d_in[6], (const u16*)d_in[1], (const u16*)d_in[2],
                                      (const u16*)d_in[4], (const u16*)d_in[5], (char*)d_ws, (u16*)d_out);
}
```

```cpp
#define BAR3B 0
#include <hip/hip_runtime.h>
#include <hip/hip_bf16.h>
#include <stdint.h>

typedef unsigned short u16;
typedef unsigned int u32;
typedef unsigned long long u64;
typedef __attribute__((ext_vector_type(8))) __bf16 bf16x8;
typedef __attribute__((ext_vector_type(2))) __bf16 bf16x2;
typedef __attribute__((ext_vector_type(4))) short s16x4;
typedef __attribute__((ext_vector_type(4))) float f32x4;
typedef __attribute__((ext_vector_type(2))) float f32x2;
typedef __attribute__((ext_vector_type(4))) unsigned u32x4;
typedef __attribute__((ext_vector_type(2))) unsigned u32x2;
typedef __attribute__((ext_vector_type(3))) unsigned u32x3;
#define LDSP __attribute__((address_space(3)))
#define DI __device__ __forceinline__
#define TOUCH 1
#define TNT ""
#define WH1 4
#define DR2 0
#define NOW 0

DI u32 pk(float lo, float hi) {
    f32x2 t = {lo, hi};
    return __builtin_bit_cast(u32, __builtin_convertvector(t, bf16x2));
}
DI u32x4 pk4(f32x4 a, f32x4 b) { return u32x4{pk(a[0], b[0]), pk(a[1], b[1]), pk(a[2], b[2]), pk(a[3], b[3])}; }
DI float bfr(float v) { return (float)(__bf16)v; }
DI f32x4 mma(u32x4 a, u32x4 b, f32x4 c) {
    return __builtin_amdgcn_mfma_f32_16x16x32_bf16(__builtin_bit_cast(bf16x8, a), __builtin_bit_cast(bf16x8, b), c, 0, 0, 0);
}
DI f32x4 mma16(u32x2 a, u32x2 b, f32x4 c) {
    return __builtin_amdgcn_mfma_f32_16x16x16bf16_1k(__builtin_bit_cast(s16x4, a), __builtin_bit_cast(s16x4, b), c, 0, 0, 0);
}
DI float dot2(u32 a, u32 b, float c) {
    return __builtin_amdgcn_fdot2_f32_bf16(__builtin_bit_cast(bf16x2, a), __builtin_bit_cast(bf16x2, b), c, false);
}
DI float cosr(float x) { return __builtin_amdgcn_cosf(x); }
DI float sinr(float x) { return __builtin_amdgcn_sinf(x); }
DI float lo16(u32 v) { return __builtin_bit_cast(float, v << 16); }
DI float hi16(u32 v) { return __builtin_bit_cast(float, v & 0xffff0000u); }
DI f32x4 lo4(u32x4 v) { return f32x4{lo16(v[0]), lo16(v[1]), lo16(v[2]), lo16(v[3])}; }
DI f32x4 hi4(u32x4 v) { return f32x4{hi16(v[0]), hi16(v[1]), hi16(v[2]), hi16(v[3])}; }
DI u32x2 trd(const LDSP char* p) {
    return __builtin_bit_cast(u32x2, __builtin_amdgcn_ds_read_tr16_b64_v4i16((LDSP s16x4*)p));
}
template <int CTRL, int RMASK> DI float dpp_add(float v) {
    const int t = __builtin_amdgcn_update_dpp(0, __builtin_bit_cast(int, v), CTRL, RMASK, 0xf, false);
    return v + __builtin_bit_cast(float, t);
}
#define BAR() asm volatile("s_waitcnt lgkmcnt(0)\n\ts_barrier" ::: "memory")
#define PIN() __builtin_amdgcn_sched_barrier(0)
#define KEEP(x) asm volatile("" : "+v"(x))

static constexpr u32 TAG = 0xFFAB7F95u;
static constexpr int SC1 = 16;
static constexpr int ROW = 32768 + 128;
static constexpr int XOFF = 256 * ROW;
static constexpr int WS_BYTES = XOFF + 8 * 2048;
static constexpr int ZP = 33 * 128;
static constexpr int WA = 4 * 2080 + 1024;
static constexpr int L_XS = 0;
static constexpr int L_ZS = 16384;
static constexpr int L_WT = L_ZS + 4 * ZP;
static constexpr int L_T1 = L_WT + 4 * WA;
static constexpr int L_T2 = L_T1 + 4 * 4224;
static constexpr int L_KC = L_T2 + 32768;
static constexpr int L_G = L_KC + 12288;
static constexpr int L_DUM = L_G + 64;
static constexpr int L_END = L_DUM + 256;
static constexpr unsigned SPIN_CAP = 1u << 16;
static constexpr u32 NEG2 = 0x80008000u;

struct K2C {
    u32x4 F1r, F1i, F2r, F2i, G1r, G1i, G2r, G2i;
    f32x4 t1c, t1s, t2c, t2s;
};
DI void k2_read(K2C& k, const LDSP char* kc) {
    k.F1r = *(const LDSP u32x4*)(kc); k.F1i = *(const LDSP u32x4*)(kc + 1024);
    k.t1c = *(const LDSP f32x4*)(kc + 2048); k.t1s = *(const LDSP f32x4*)(kc + 3072);
    k.F2r = *(const LDSP u32x4*)(kc + 4096); k.F2i = *(const LDSP u32x4*)(kc + 5120);
    k.G1r = *(const LDSP u32x4*)(kc + 6144); k.G1i = *(const LDSP u32x4*)(kc + 7168);
    k.t2c = *(const LDSP f32x4*)(kc + 8192); k.t2s = *(const LDSP f32x4*)(kc + 9216);
    k.G2r = *(const LDSP u32x4*)(kc + 10240); k.G2i = *(const LDSP u32x4*)(kc + 11264);
    KEEP(k.F1r); KEEP(k.F1i); KEEP(k.F2r); KEEP(k.F2i); KEEP(k.G1r); KEEP(k.G1i); KEEP(k.G2r); KEEP(k.G2i);
    KEEP(k.t1c); KEEP(k.t1s); KEEP(k.t2c); KEEP(k.t2s);
}
DI u32x4 k2_inv(const K2C& k, f32x4 Xr, f32x4 Xi, f32x4 wr, f32x4 wi) {
    const f32x4 z4 = {0.f, 0.f, 0.f, 0.f};
    const u32x4 y = pk4(Xr * wr - Xi * wi, Xr * wi + Xi * wr);
    const f32x4 er = mma(y, k.G1r, z4), ei = mma(y, k.G1i, z4);
    const u32x4 b = pk4(er * k.t2c - ei * k.t2s, ei * k.t2c + er * k.t2s);
    return pk4(mma(k.G2r, b, z4), mma(k.G2i, b, z4));
}

typedef __attribute__((ext_vector_type(8))) unsigned long long u64x8;

__global__ __launch_bounds__(576) void gf_fused(const u16* x_, const u16* gw_, const u16* w0_, const u16* w1_,
                                                const u16* w2_, const u16* w3_, char* ws_, u16* out_) {
    __shared__ __attribute__((aligned(16))) char smem[L_END];
    LDSP char* sm = (LDSP char*)smem;
    u64x8 ka;
    asm volatile("s_load_dwordx16 %0, %1, 0x0" : "=s"(ka) : "s"(__builtin_amdgcn_kernarg_segment_ptr()));
    PIN();
    const int tid = threadIdx.x, lane = tid & 63;
    const int wv = __builtin_amdgcn_readfirstlane(tid >> 6);
    const int p = wv >> 1, h = wv & 1;
    const int q = lane >> 4, n = lane & 15;
    const int id = blockIdx.x;
    const int s1 = (id & 7) * 32 + (id >> 3);
    const int r = id & 7, c = id >> 3;
    const f32x4 z4 = {0.f, 0.f, 0.f, 0.f};
    float c4[4], s4[4];
#pragma unroll
    for (int i = 0; i < 4; ++i) {
        const float a = (float)((4 * q + i) * n) * (1.0f / 32.0f);
        c4[i] = cosr(a); s4[i] = sinr(a);
        KEEP(c4[i]); KEEP(s4[i]);
    }
    int gvo = (lane ^ (((lane >> 4) & 1) << 1)) * 16;
    KEEP(gvo);
    PIN();
    asm volatile("s_waitcnt lgkmcnt(0)" : "+s"(ka));
    PIN();
    const u16* x = (const u16*)ka[0];
    const u16* gw = (const u16*)ka[1];
    const u16* w0 = (const u16*)ka[2];
    const u16* w1 = (const u16*)ka[3];
    const u16* w2 = (const u16*)ka[4];
    const u16* w3 = (const u16*)ka[5];
    char* ws = (char*)ka[6];
    u16* out = (u16*)ka[7];
    const __amdgpu_buffer_rsrc_t rs = __builtin_amdgcn_make_buffer_rsrc(ws, 0, WS_BYTES, 0x00020000);

    if (wv == 8) {
        if (c != 31) return;
        u32 last[4];
#pragma unroll
        for (int j = 0; j < 4; ++j) last[j] = *(const __attribute__((address_space(1))) u32*)((j == 0 ? w0 : j == 1 ? w1 : j == 2 ? w2 : w3) + 2048 * 513 - 2);
        BAR(); BAR(); BAR();
        K2C k;
        k2_read(k, sm + L_KC + lane * 16);
        const int xo = XOFF + r * 2048 + ((8 * ((n >> 1) & 1) + 2 * (n >> 2) + (n & 1)) + 16 * q) * 32;
        u32x4 xa[2];
#pragma unroll
        for (int e = 0; e < 2; ++e) xa[e] = __builtin_amdgcn_raw_buffer_load_b128(rs, xo + e * 16, 0, SC1);
        PIN();
        BAR();
        f32x4 wf[4];
#pragma unroll
        for (int j = 0; j < 4; ++j) {
            wf[j] = lo4(*(const LDSP u32x4*)(sm + L_WT + j * WA + 8320 + lane * 16));
            if (r == 7 && lane == 63) wf[j][3] = hi16(last[j]);
        }
        for (unsigned spins = 0;;) {
            u32 bad = 0;
#pragma unroll
            for (int e = 0; e < 2; ++e) bad |= (xa[e][1] ^ TAG) | (xa[e][3] ^ TAG);
            if (__all(bad == 0) || ++spins > SPIN_CAP) break;
#pragma unroll
            for (int e = 0; e < 2; ++e) xa[e] = __builtin_amdgcn_raw_buffer_load_b128(rs, xo + e * 16, 0, SC1);
        }
        const u32x4 f = {xa[0][0], xa[0][2], xa[1][0], xa[1][2]};
        const f32x4 er = mma(f, k.F1r, z4), ei = mma(f, k.F1i, z4);
        const u32x4 b = pk4(er * k.t1c + ei * k.t1s, ei * k.t1c - er * k.t1s);
        const f32x4 Xr = mma(k.F2r, b, z4), Xi = mma(k.F2i, b, z4);
        const u32x4 oa = k2_inv(k, Xr, Xi, wf[0], wf[1]), ob = k2_inv(k, Xr, Xi, wf[2], wf[3]);
        const int vs = XOFF + r * 2048 + lane * 32;
        __builtin_amdgcn_raw_buffer_store_b128(u32x4{oa[0], ob[0], oa[1], ob[1]}, rs, vs, 0, SC1);
        __builtin_amdgcn_raw_buffer_store_b128(u32x4{oa[2], ob[2], oa[3], ob[3]}, rs, vs + 16, 0, SC1);
        BAR();
        return;
    }
    {
        const u16* r0 = x + (size_t)(s1 + 256 * wv) * 1024;
        const u32 l0 = (u32)(size_t)(sm + L_XS) + wv * 2048;
        u32 keep;
        asm volatile("s_mov_b32 %0, m0\n\ts_mov_b32 m0, %3\n\ts_nop 0\n\t"
                     "global_load_lds_dwordx4 %1, %2\n\tglobal_load_lds_dwordx4 %1, %2 offset:1024\n\t"
                     "s_mov_b32 m0, %0"
                     : "=&s"(keep) : "v"(gvo), "s"(r0), "s"(l0) : "memory");
    }
    PIN();
    const __amdgpu_buffer_rsrc_t rg = __builtin_amdgcn_make_buffer_rsrc((void*)gw, 0, 2048, 0x00020000);
    const u32x4 gw0 = __builtin_amdgcn_raw_buffer_load_b128(rg, gvo, 0, 0);
    const u32x4 gw1 = __builtin_amdgcn_raw_buffer_load_b128(rg, gvo + 1024, 0, 0);
    PIN();
    const u16* wp = p == 0 ? w0 : p == 1 ? w1 : p == 2 ? w2 : w3;
    const int xpos = XOFF + (((s1 & 15) + 16 * (s1 >> 6)) * 4 + ((s1 >> 4) & 3)) * 8;
    const int rho = 16 * h + n;
    const u32 mn = (n & 1) ? NEG2 : 0u;

    f32x4 tc[2], ts[2];
#pragma unroll
    for (int T = 0; T < 2; ++T)
#pragma unroll
        for (int i = 0; i < 4; ++i) {
            const float a = (float)((16 * T + 4 * q + i) * rho) * (1.0f / 1024.0f);
            tc[T][i] = cosr(a); ts[T][i] = sinr(a);
        }
    int zdo = L_ZS + q * ZP + rho * 128 + ((p ^ (rho & 7)) << 4);
    int zmo = L_ZS + q * ZP + (32 - rho) * 128 + (((7 - p) ^ ((32 - rho) & 7)) << 4);
    {
        const u32 c01 = pk(c4[0], c4[1]), c23 = pk(c4[2], c4[3]), s01 = pk(s4[0], s4[1]), s23 = pk(s4[2], s4[3]);
        const u32 mh = h ? 0x80000000u : 0u;
        u32x4 Cf = {c01 ^ mh, c23 ^ mh, c01 ^ mh ^ mn, c23 ^ mh ^ mn};
        u32x4 Sf = {s01 ^ mh, s23 ^ mh, s01 ^ mh ^ mn, s23 ^ mh ^ mn};
        u32x4 Nf = Sf ^ NEG2;
        u32x4 Are[2][2], Aim[2][2];
#pragma unroll
        for (int Tp = 0; Tp < 2; ++Tp)
#pragma unroll
            for (int T = 0; T < 2; ++T) {
                const u32 m = (Tp ? 0x80000000u : 0u) ^ (T ? mn : 0u);
                Are[Tp][T] = u32x4{c01 ^ m, c23 ^ m, s01 ^ m, s23 ^ m};
                Aim[Tp][T] = u32x4{s01 ^ m ^ NEG2, s23 ^ m ^ NEG2, c01 ^ m, c23 ^ m};
                KEEP(Are[Tp][T]); KEEP(Aim[Tp][T]);
            }
        u32x2 Sep;
        {
            const int rr = n >> 1, part = n & 1, sg = (rr & 1) ? -1 : 1;
            const float aa = (float)(q * rr + sg) * 0.125f, ab = (float)(q * rr - sg) * 0.125f;
            const float R2 = 1.41421356237f;
            const float ar = R2 * cosr(aa), ai = -R2 * sinr(aa), br = R2 * cosr(ab), bi = -R2 * sinr(ab);
            Sep[0] = part ? pk(ai, ar) : pk(ar, -ai);
            Sep[1] = part ? pk(bi, -br) : pk(br, bi);
        }
        KEEP(Cf); KEEP(Sf); KEEP(Nf); KEEP(Sep); KEEP(tc[0]); KEEP(tc[1]); KEEP(ts[0]); KEEP(ts[1]);
        const LDSP char* xs = sm + L_XS + p * 2048;
        const LDSP char* xb0 = xs + 64 * (4 * q + (n >> 2)) + 32 * (q & 1) + 8 * (n & 3);
        const LDSP char* xb1 = xs + 64 * (4 * q + (n >> 2)) + 32 * ((q & 1) ^ 1) + 8 * (n & 3);
        LDSP char* zw = sm + L_ZS + p * ZP + rho * 128;
        int vo = s1 * ROW + (n >> 1) * 4096 + (h + 8 * p) * 128 + q * 32 + (n & 1) * 16;
        u32x4 g1[4];
#pragma unroll
        for (int e = 0; e < 4; ++e) { g1[e] = u32x4{0u, TAG, 0u, TAG}; KEEP(g1[e]); }
        KEEP(vo); KEEP(zdo); KEEP(zmo);
        PIN();
        {
            const u32 vw = (r + 8 * (8 * c + 4 * h + q)) * 1026 + min(n * 128, 1024);
            const u32 l1 = (u32)(size_t)(sm + L_DUM);
            u32 keep;
            asm volatile("s_mov_b32 %0, m0\n\ts_mov_b32 m0, %3\n\ts_nop 0\n\tglobal_load_lds_ushort %1, %2" TNT "\n\ts_mov_b32 m0, %0"
                         : "=&s"(keep) : "v"(vw), "s"(wp), "s"(l1) : "memory");
        }
        PIN();
        asm volatile("s_waitcnt vmcnt(%0)\n\ts_barrier" :: "n"(2 + (TOUCH == 1)) : "memory");
        PIN();
        u32x4 Bd[2];
#pragma unroll
        for (int T = 0; T < 2; ++T) {
            const LDSP char* b = T ? xb1 : xb0;
            const u32x2 a0 = trd(b), a1 = trd(b + 1024), a2 = trd(b + 8192), a3 = trd(b + 9216);
            const u32x4 xr = {a0[0], a0[1], a1[0], a1[1]}, xi = {a2[0], a2[1], a3[0], a3[1]};
            f32x4 Dr = mma(xr, Cf, z4); Dr = mma(xi, Sf, Dr);
            f32x4 Di = mma(xr, Nf, z4); Di = mma(xi, Cf, Di);
            const f32x4 re = Dr * tc[T] + Di * ts[T], im = Di * tc[T] - Dr * ts[T];
            Bd[T] = u32x4{pk(re[0], re[1]), pk(re[2], re[3]), pk(im[0], im[1]), pk(im[2], im[3])};
        }
#pragma unroll
        for (int Tp = 0; Tp < 2; ++Tp) {
            f32x4 Zr = mma(Are[Tp][0], Bd[0], z4); Zr = mma(Are[Tp][1], Bd[1], Zr);
            f32x4 Zi = mma(Aim[Tp][0], Bd[0], z4); Zi = mma(Aim[Tp][1], Bd[1], Zi);
            const u32x4 o = pk4(Zr, Zi);
            *(LDSP u32x4*)(zw + (((4 * Tp + q) ^ (rho & 7)) << 4)) = o;
            if (rho == 0) {
                if (Tp == 1) {
#pragma unroll
                    for (int i = 0; i < 4; ++i) *(LDSP u32*)(zw + 32 * 128 + ((15 + 4 * q + i) << 2)) = o[i];
                } else if (q == 0) {
                    *(LDSP u32*)(zw + 32 * 128 + 31 * 4) = o[0];
                }
            }
        }
        BAR();
        {
            const u32x4 dv = *(const LDSP u32x4*)(sm + zdo);
            const u32x4 mv = *(const LDSP u32x4*)(sm + zmo);
            f32x4 d[4];
#pragma unroll
            for (int e = 0; e < 4; ++e) d[e] = mma16(u32x2{dv[e], mv[3 - e]}, Sep, z4);
            PIN();
#pragma unroll
            for (int e = 0; e < 4; ++e) {
                g1[e][0] = pk(d[e][0], d[e][1]); g1[e][2] = pk(d[e][2], d[e][3]);
                __builtin_amdgcn_raw_buffer_store_b128(g1[e], rs, vo + e * 256, 0, SC1);
            }
            if (wv == 6) {
                const u32 v = *(const LDSP u32*)(sm + L_ZS + q * ZP + 64);
                const f32x4 dx = mma16(Sep, u32x2{v, v}, z4);
                if (n < 2) __builtin_amdgcn_raw_buffer_store_b64(u32x2{n ? pk(dx[2], dx[3]) : pk(dx[0], dx[1]), TAG}, rs, xpos + (2 * q + n) * 2048, 0, SC1);
            }
        }
        PIN();
    }
    if (wv < 6) {
        const bool ua = (wv == 1) || (wv == 2);
        const int be = wv == 0 ? 16 * r : (wv == 1 ? r : 0);
        const int ga = (wv == 1 || wv == 4) ? 8 : ((wv == 3 || wv == 5) ? -128 : 128);
        const int de = wv == 4 ? r : (wv == 5 ? -16 * r : 0);
        const float u = (float)(be + ga * n) * (1.0f / 2048.0f), w = (float)(de * n) * (1.0f / 2048.0f);
        f32x4 cc, ss;
#pragma unroll
        for (int i = 0; i < 4; ++i) {
            const int al = ua ? 8 * (i >> 1) + 2 * q + (i & 1) : 4 * q + i;
            const float a = (float)al * u + w;
            cc[i] = cosr(a); ss[i] = sinr(a);
        }
        u32x4 o0 = __builtin_bit_cast(u32x4, cc), o1 = __builtin_bit_cast(u32x4, ss);
        if (wv != 1 && wv != 4) { o0 = pk4(cc, ss); o1 = pk4(-ss, cc); }
        *(LDSP u32x4*)(sm + L_KC + wv * 2048 + lane * 16) = o0;
        *(LDSP u32x4*)(sm + L_KC + wv * 2048 + 1024 + lane * 16) = o1;
    }
    const __amdgpu_buffer_rsrc_t rw = __builtin_amdgcn_make_buffer_rsrc((void*)wp, 0, NOW ? 0 : 2048 * 513 * 2, 0x00020000);
    int wvo = ((r + 8 * (q + 16 * ((lane >> 2) & 3) + 128 * h)) * 513 + 16 * c + 4 * (lane & 3)) * 2;
    int lo2 = (32 * wv + (lane >> 3)) * ROW + (r * 32 + c) * 128 + (lane & 7) * 16;
    KEEP(wvo); KEEP(lo2);
    u32x3 wl[8];
#define WLOADS(lo, hi) do { PIN(); \
        _Pragma("unroll") for (int e = lo; e < hi; ++e) \
            wl[e] = __builtin_amdgcn_raw_buffer_load_b96(rw, wvo, (4 * (e & 3) + 64 * (e >> 2)) * 8 * 513 * 2, 0); \
        PIN(); } while (0)
#define WSTAGE(lo, hi) do { \
        _Pragma("unroll") for (int e = lo; e < hi; ++e) { \
            *(LDSP u32*)(wt + (16 * (e & 3) + 64 * (e >> 2)) * 4) = wl[e][0]; \
            *(LDSP u32*)(wt + 1024 + (16 * (e & 3) + 64 * (e >> 2)) * 4) = wl[e][1]; } \
        if (c == 31 && (lane & 3) == 3) { \
            _Pragma("unroll") for (int e = lo; e < hi; ++e) *(LDSP u32*)(wt + 2080 + (16 * (e & 3) + 64 * (e >> 2)) * 4) = wl[e][2]; } \
        } while (0)
    BAR();
    WLOADS(0, WH1);
    K2C k;
    int so2[4];
    {
        u32x4 a[4];
#pragma unroll
        for (int e = 0; e < 4; ++e) a[e] = __builtin_amdgcn_raw_buffer_load_b128(rs, lo2, e * 8 * ROW, SC1);
        PIN();
        WLOADS(WH1, 8);
        k2_read(k, sm + L_KC + lane * 16);
        {
            const LDSP char* xs = sm + L_XS + wv * 2048 + lane * 16;
            const u32x4 x0 = *(const LDSP u32x4*)(xs), x1 = *(const LDSP u32x4*)(xs + 1024);
            float acc = 0.f, acc2 = 0.f;
#pragma unroll
            for (int i = 0; i < 4; ++i) { acc = dot2(x0[i], gw0[i], acc); acc2 = dot2(x1[i], gw1[i], acc2); }
            acc += acc2;
            acc = dpp_add<0x111, 0xf>(acc); acc = dpp_add<0x112, 0xf>(acc); acc = dpp_add<0x114, 0xf>(acc); acc = dpp_add<0x118, 0xf>(acc);
            acc = dpp_add<0x142, 0xa>(acc); acc = dpp_add<0x143, 0xc>(acc);
            const float z = bfr(acc);
            const float g = bfr(__builtin_amdgcn_rcpf(1.0f + __expf(-z)));
            const float og = bfr(1.0f - g);
            if (lane == 63) {
                LDSP float* G = (LDSP float*)(sm + L_G) + (wv & 3) * 4 + (wv >> 2);
                G[0] = g; G[2] = og;
            }
        }
#pragma unroll
        for (int e = 0; e < 4; ++e) { so2[e] = (lo2 ^ ((lane >> 3) << 4)) + e * 8 * ROW; KEEP(so2[e]); }
        LDSP char* wt = sm + L_WT + p * WA + (lane & 3) * 2080 + (q * 4 + ((lane >> 2) & 3) + 128 * h) * 4;
        const int qd = (lane >> 1) & 3;
        int t1o = L_T1 + qd * 4224 + (qd & 1) * 16 + (qd >> 1) * 64 + h * 512 + p * 128 + (lane & 1) * 32 + (lane >> 3) * 4;
        KEEP(t1o);
        WSTAGE(0, WH1);
        PIN();
        u32 bad = 0;
#pragma unroll
        for (int e = 0; e < 4; ++e) bad |= (a[e][1] ^ TAG) | (a[e][3] ^ TAG);
        if (!__all(bad == 0)) {
            for (unsigned spins = 0; spins < SPIN_CAP; ++spins) {
                if (bad) {
#pragma unroll
                    for (int e = 0; e < 4; ++e) a[e] = __builtin_amdgcn_raw_buffer_load_b128(rs, lo2, e * 8 * ROW, SC1);
                }
                bad = 0;
#pragma unroll
                for (int e = 0; e < 4; ++e) bad |= (a[e][1] ^ TAG) | (a[e][3] ^ TAG);
                if (__all(bad == 0)) break;
            }
        }
#pragma unroll
        for (int e = 0; e < 4; ++e) {
            *(LDSP u32*)(sm + t1o + (e & 1) * 1024 + (e >> 1) * 64) = a[e][0];
            *(LDSP u32*)(sm + t1o + 2048 + (e & 1) * 1024 + (e >> 1) * 64) = a[e][2];
        }
        PIN();
        WSTAGE(WH1, 8);
    }
    BAR();
    {
        const LDSP char* t1 = sm + L_T1 + p * 4224 + (p & 1) * 16 + (p >> 1) * 64 + h * 2048 + lane * 8;
        u32x4 fa[2];
#pragma unroll
        for (int t = 0; t < 2; ++t) {
            const u32x2 a0 = trd(t1 + t * 1024), a1 = trd(t1 + t * 1024 + 512);
            fa[t] = u32x4{a0[0], a0[1], a1[0], a1[1]};
        }
        u32x4 wq[4];
        {
            const LDSP char* wt = sm + L_WT + p * 2080 + h * 1024 + lane * 16;
#pragma unroll
            for (int j = 0; j < 4; ++j) wq[j] = *(const LDSP u32x4*)(wt + j * WA);
        }
        PIN();
        f32x4 dr[2], di[2];
#pragma unroll
        for (int t = 0; t < 2; ++t) {
            const f32x4 er = mma(fa[t], k.F1r, z4), ei = mma(fa[t], k.F1i, z4);
            const f32x4 c4v = {k.t1c[2 * t], k.t1c[2 * t], k.t1c[2 * t + 1], k.t1c[2 * t + 1]};
            const f32x4 s4v = {k.t1s[2 * t], k.t1s[2 * t], k.t1s[2 * t + 1], k.t1s[2 * t + 1]};
            dr[t] = er * c4v + ei * s4v; di[t] = ei * c4v - er * s4v;
        }
        u32x4 o[2][2];
#pragma unroll
        for (int u = 0; u < 2; ++u) {
            const u32x4 b = {pk(dr[0][u], di[0][u]), pk(dr[0][2 + u], di[0][2 + u]), pk(dr[1][u], di[1][u]), pk(dr[1][2 + u], di[1][2 + u])};
            const f32x4 Xr = mma(k.F2r, b, z4), Xi = mma(k.F2i, b, z4);
            o[0][u] = k2_inv(k, Xr, Xi, u ? hi4(wq[0]) : lo4(wq[0]), u ? hi4(wq[1]) : lo4(wq[1]));
            o[1][u] = k2_inv(k, Xr, Xi, u ? hi4(wq[2]) : lo4(wq[2]), u ? hi4(wq[3]) : lo4(wq[3]));
        }
        LDSP char* t2 = sm + L_T2 + (n + 64 * q) * 128 + ((wv ^ (n & 7)) << 4);
#pragma unroll
        for (int i = 0; i < 4; ++i)
            *(LDSP u32x4*)(t2 + i * 2048) = u32x4{o[0][0][i], o[1][0][i], o[0][1][i], o[1][1][i]};
        const LDSP char* t2r = sm + L_T2 + wv * 4096 + lane * 16;
        BAR();
        u32x4 tv[4];
#pragma unroll
        for (int e = 0; e < 4; ++e) tv[e] = *(const LDSP u32x4*)(t2r + e * 1024);
        PIN();
#pragma unroll
        for (int e = 0; e < 4; ++e) __builtin_amdgcn_raw_buffer_store_b128(tv[e], rs, so2[e], 0, SC1);
    }
    u32x4 Fr[2][2], Fi[2][2], Er_[2], Ei_[2], Mix;
    {
        u32 fr[4], fi[4];
#pragma unroll
        for (int i = 0; i < 4; ++i) { fr[i] = pk(c4[i], -s4[i]); fi[i] = pk(s4[i], c4[i]); }
#pragma unroll
        for (int Tp = 0; Tp < 2; ++Tp)
#pragma unroll
            for (int T = 0; T < 2; ++T) {
                const u32 m0 = T ? mn : 0u, m1 = m0 ^ (Tp ? NEG2 : 0u);
                Fr[Tp][T] = u32x4{fr[0] ^ m0, fr[1] ^ m1, fr[2] ^ m0, fr[3] ^ m1};
                Fi[Tp][T] = u32x4{fi[0] ^ m0, fi[1] ^ m1, fi[2] ^ m0, fi[3] ^ m1};
                KEEP(Fr[Tp][T]); KEEP(Fi[Tp][T]);
            }
#pragma unroll
        for (int s = 0; s < 2; ++s) {
            const u32 m0 = s ? mn : 0u, m1 = m0 ^ (h ? NEG2 : 0u);
            Er_[s] = u32x4{fr[0] ^ m0, fr[1] ^ m1, fr[2] ^ m0, fr[3] ^ m1};
            Ei_[s] = u32x4{fi[0] ^ m0, fi[1] ^ m1, fi[2] ^ m0, fi[3] ^ m1};
            KEEP(Er_[s]); KEEP(Ei_[s]);
        }
        const f32x4 G = *(const LDSP f32x4*)(sm + L_G + (n >> 2) * 16);
        const int pp = n >> 2, i = n & 3;
        const float sc = 1.0f / 4194304.0f;
#pragma unroll
        for (int rr = 0; rr < 2; ++rr) {
            const float a = (float)(pp * (2 * q + rr)) * 0.125f;
            const float cc = cosr(a) * sc, ss = sinr(a) * sc;
            const float tau = ((rr == 0) == (i < 2)) ? 1.f : -1.f;
#pragma unroll
            for (int f = 0; f < 2; ++f) {
                const float g0 = G[2 * f], g1 = tau * G[2 * f + 1];
                const float cr = cc * g0 - ss * g1, ci = ss * g0 + cc * g1;
                Mix[2 * rr + f] = (i & 1) ? (i == 1 ? pk(ci, cr) : pk(-ci, -cr)) : pk(cr, -ci);
            }
        }
        KEEP(Mix);
    }
    PIN();
    asm volatile("s_waitcnt vmcnt(%0)" :: "n"(DR2) : "memory");
    PIN();
    {
        const int vo = s1 * ROW + (2 * q) * 4096 + (rho + 128 * p) * 8;
        const int vx = xpos + q * 4096;
        u32x2 a[8], b0 = {0u, 0u}, b1 = {0u, 0u};
        bool ok = false;
        for (unsigned spins = 0;;) {
            if (!ok) {
#pragma unroll
                for (int e = 0; e < 8; ++e) a[e] = __builtin_amdgcn_raw_buffer_load_b64(rs, vo + (e >> 1) * 256, (e & 1) * 4096, SC1);
                if (wv == 6) {
                    b0 = __builtin_amdgcn_raw_buffer_load_b64(rs, vx, 0, SC1);
                    b1 = __builtin_amdgcn_raw_buffer_load_b64(rs, vx + 2048, 0, SC1);
                }
            }
            ok = b0[1] != TAG && b1[1] != TAG;
#pragma unroll
            for (int e = 0; e < 8; ++e) ok = ok & (a[e][1] != TAG);
            if (__all(ok) || ++spins > SPIN_CAP) break;
        }
        LDSP char* zq = sm + L_ZS + q * ZP;
        u32x4 dv, mv;
#pragma unroll
        for (int e = 0; e < 4; ++e) {
            f32x4 d = mma(Mix, u32x4{a[2 * e][0], a[2 * e][1], a[2 * e + 1][0], a[2 * e + 1][1]}, z4);
            if (e == 0 && wv == 0 && n == 0) { d[0] = 0.5f * (d[0] + d[2]); d[1] = 0.5f * (d[1] + d[3]); }
            dv[e] = pk(d[0], d[1]);
            mv[3 - e] = pk(d[2], d[3]);
        }
        *(LDSP u32x4*)(sm + zdo) = dv;
        *(LDSP u32x4*)(sm + zmo) = mv;
        if (rho == 0) {
#pragma unroll
            for (int e = 0; e < 4; ++e) {
                const int kk = 32 - 4 * p - e;
                if (kk < 32) *(LDSP u32*)(zq + kk * 4) = mv[3 - e];
            }
        }
        if (wv == 6) {
            const f32x4 d = mma(Mix, u32x4{b0[0], b0[1], b1[0], b1[1]}, z4);
            if (n == 0) *(LDSP u32*)(zq + 64) = pk(0.5f * (d[0] + d[2]), 0.5f * (d[1] + d[3]));
        }
    }
    BAR();
    {
        const LDSP char* zp = sm + L_ZS + p * ZP + n * 128;
        u32x4 Ad[2];
#pragma unroll
        for (int T = 0; T < 2; ++T) {
            const u32x4 a0 = *(const LDSP u32x4*)(zp + T * 2048 + ((q ^ (n & 7)) << 4));
            const u32x4 a1 = *(const LDSP u32x4*)(zp + T * 2048 + (((4 + q) ^ (n & 7)) << 4));
            f32x4 er = mma(a0, Er_[0], z4); er = mma(a1, Er_[1], er);
            f32x4 ei = mma(a0, Ei_[0], z4); ei = mma(a1, Ei_[1], ei);
            Ad[T] = pk4(er * tc[T] - ei * ts[T], ei * tc[T] + er * ts[T]);
        }
        const __amdgpu_buffer_rsrc_t ro = __builtin_amdgcn_make_buffer_rsrc(out, 0, 2048 * 1024 * 2, 0x00020000);
        const int vo = ((s1 + 256 * p) * 1024 + 16 * h + 4 * q + 32 * n) * 2;
#pragma unroll
        for (int Tp = 0; Tp < 2; ++Tp) {
            f32x4 orr = mma(Ad[0], Fr[Tp][0], z4); orr = mma(Ad[1], Fr[Tp][1], orr);
            f32x4 oi = mma(Ad[0], Fi[Tp][0], z4); oi = mma(Ad[1], Fi[Tp][1], oi);
            __builtin_amdgcn_raw_buffer_store_b64(u32x2{pk(orr[0], orr[1]), pk(orr[2], orr[3])}, ro, vo + 1024 * Tp, 0, 0);
            __builtin_amdgcn_raw_buffer_store_b64(u32x2{pk(oi[0], oi[1]), pk(oi[2], oi[3])}, ro, vo + 1024 * Tp, 2 << 20, 0);
        }
    }
}

extern "C" void kernel_launch(void* const* d_in, const int* in_sizes, int n_in,
                              void* d_out, int out_size, void* d_ws, size_t ws_size,
                              hipStream_t stream) {
    gf_fused<<<256, 576, 0, stream>>>((const u16*)d_in[0], (const u16*)d_in[6], (const u16*)d_in[1], (const u16*)d_in[2],
                                      (const u16*)d_in[4], (const u16*)d_in[5], (char*)d_ws, (u16*)d_out);
}
```

```cpp
#define WE0 2
#define WE1 4
#define WE2 4
#define WH1 6
#define DR1 8
#include <hip/hip_runtime.h>
#include <hip/hip_bf16.h>
#include <stdint.h>

typedef unsigned short u16;
typedef unsigned int u32;
typedef unsigned long long u64;
typedef __attribute__((ext_vector_type(8))) __bf16 bf16x8;
typedef __attribute__((ext_vector_type(2))) __bf16 bf16x2;
typedef __attribute__((ext_vector_type(4))) short s16x4;
typedef __attribute__((ext_vector_type(4))) float f32x4;
typedef __attribute__((ext_vector_type(2))) float f32x2;
typedef __attribute__((ext_vector_type(4))) unsigned u32x4;
typedef __attribute__((ext_vector_type(2))) unsigned u32x2;
typedef __attribute__((ext_vector_type(3))) unsigned u32x3;
#define LDSP __attribute__((address_space(3)))
#define DI __device__ __forceinline__
#define BAR3 1
#define DR2 0
#define NOW 0

DI u32 pk(float lo, float hi) {
    f32x2 t = {lo, hi};
    return __builtin_bit_cast(u32, __builtin_convertvector(t, bf16x2));
}
DI u32x4 pk4(f32x4 a, f32x4 b) { return u32x4{pk(a[0], b[0]), pk(a[1], b[1]), pk(a[2], b[2]), pk(a[3], b[3])}; }
DI float bfr(float v) { return (float)(__bf16)v; }
DI f32x4 mma(u32x4 a, u32x4 b, f32x4 c) {
    return __builtin_amdgcn_mfma_f32_16x16x32_bf16(__builtin_bit_cast(bf16x8, a), __builtin_bit_cast(bf16x8, b), c, 0, 0, 0);
}
DI f32x4 mma16(u32x2 a, u32x2 b, f32x4 c) {
    return __builtin_amdgcn_mfma_f32_16x16x16bf16_1k(__builtin_bit_cast(s16x4, a), __builtin_bit_cast(s16x4, b), c, 0, 0, 0);
}
DI float dot2(u32 a, u32 b, float c) {
    return __builtin_amdgcn_fdot2_f32_bf16(__builtin_bit_cast(bf16x2, a), __builtin_bit_cast(bf16x2, b), c, false);
}
DI float cosr(float x) { return __builtin_amdgcn_cosf(x); }
DI float sinr(float x) { return __builtin_amdgcn_sinf(x); }
DI float lo16(u32 v) { return __builtin_bit_cast(float, v << 16); }
DI float hi16(u32 v) { return __builtin_bit_cast(float, v & 0xffff0000u); }
DI f32x4 lo4(u32x4 v) { return f32x4{lo16(v[0]), lo16(v[1]), lo16(v[2]), lo16(v[3])}; }
DI f32x4 hi4(u32x4 v) { return f32x4{hi16(v[0]), hi16(v[1]), hi16(v[2]), hi16(v[3])}; }
DI u32x2 trd(const LDSP char* p) {
    return __builtin_bit_cast(u32x2, __builtin_amdgcn_ds_read_tr16_b64_v4i16((LDSP s16x4*)p));
}
template <int CTRL, int RMASK> DI float dpp_add(float v) {
    const int t = __builtin_amdgcn_update_dpp(0, __builtin_bit_cast(int, v), CTRL, RMASK, 0xf, false);
    return v + __builtin_bit_cast(float, t);
}
#define BAR() asm volatile("s_waitcnt lgkmcnt(0)\n\ts_barrier" ::: "memory")
#define PIN() __builtin_amdgcn_sched_barrier(0)
#define KEEP(x) asm volatile("" : "+v"(x))

static constexpr u32 TAG = 0xFFAB7F95u;
static constexpr int SC1 = 16;
static constexpr int ROW = 32768 + 128;
static constexpr int XOFF = 256 * ROW;
static constexpr int YOFF = XOFF + 8 * 2048;
static constexpr int WS_BYTES = YOFF + 8 * 4096;
static constexpr int ZP = 33 * 128;
static constexpr int L_XS = 0;
static constexpr int L_ZS = 16384;
static constexpr int L_X1 = L_ZS + 4 * ZP;
static constexpr int L_X2 = L_X1 + 16384;
static constexpr int L_WT = L_X2 + 32768;
static constexpr int L_G = L_WT + 32768;
static constexpr int L_END = L_G + 64;
static constexpr unsigned SPIN_CAP = 1u << 16;
static constexpr u32 NEG2 = 0x80008000u;

struct K2C {
    u32x4 F1r, F1i, F2r, F2i, G1r, G1i, G2r, G2i;
    f32x4 t1c, t1s, t2c, t2s;
};
DI void frag(float a0, float da, u32x4& fr, u32x4& fi) {
#pragma unroll
    for (int i = 0; i < 4; ++i) {
        const float a = a0 + (float)i * da, cc = cosr(a), ss = sinr(a);
        fr[i] = pk(cc, ss); fi[i] = pk(-ss, cc);
    }
}
DI u32x4 k2_inv(const K2C& k, f32x4 Xr, f32x4 Xi, f32x4 wr, f32x4 wi) {
    const f32x4 z4 = {0.f, 0.f, 0.f, 0.f};
    const u32x4 y = pk4(Xr * wr - Xi * wi, Xr * wi + Xi * wr);
    const f32x4 er = mma(y, k.G1r, z4), ei = mma(y, k.G1i, z4);
    const u32x4 b = pk4(er * k.t2c - ei * k.t2s, ei * k.t2c + er * k.t2s);
    return pk4(mma(k.G2r, b, z4), mma(k.G2i, b, z4));
}

typedef __attribute__((ext_vector_type(8))) unsigned long long u64x8;

__global__ __launch_bounds__(576) void gf_fused(const u16* x_, const u16* gw_, const u16* w0_, const u16* w1_,
                                                const u16* w2_, const u16* w3_, char* ws_, u16* out_) {
    __shared__ __attribute__((aligned(16))) char smem[L_END];
    LDSP char* sm = (LDSP char*)smem;
    u64x8 ka;
    asm volatile("s_load_dwordx16 %0, %1, 0x0" : "=s"(ka) : "s"(__builtin_amdgcn_kernarg_segment_ptr()));
    PIN();
    const int tid = threadIdx.x, lane = tid & 63;
    const int wv = __builtin_amdgcn_readfirstlane(tid >> 6);
    const int p = wv >> 1, h = wv & 1;
    const int q = lane >> 4, n = lane & 15;
    const int id = blockIdx.x;
    const int s1 = (id & 7) * 32 + (id >> 3);
    const int r = id & 7, c = id >> 3;
    const f32x4 z4 = {0.f, 0.f, 0.f, 0.f};
    float c4[4], s4[4];
#pragma unroll
    for (int i = 0; i < 4; ++i) {
        const float a = (float)((4 * q + i) * n) * (1.0f / 32.0f);
        c4[i] = cosr(a); s4[i] = sinr(a);
        KEEP(c4[i]); KEEP(s4[i]);
    }
    int gvo = (lane ^ (((lane >> 4) & 1) << 1)) * 16;
    KEEP(gvo);
    PIN();
    asm volatile("s_waitcnt lgkmcnt(0)" : "+s"(ka));
    PIN();
    const u16* x = (const u16*)ka[0];
    const u16* gw = (const u16*)ka[1];
    const u16* w0 = (const u16*)ka[2];
    const u16* w1 = (const u16*)ka[3];
    const u16* w2 = (const u16*)ka[4];
    const u16* w3 = (const u16*)ka[5];
    char* ws = (char*)ka[6];
    u16* out = (u16*)ka[7];
    const __amdgpu_buffer_rsrc_t rs = __builtin_amdgcn_make_buffer_rsrc(ws, 0, WS_BYTES, 0x00020000);

    if (wv == 8) {
        const int j = lane & 3;
        const u16* wq = j == 0 ? w0 : j == 1 ? w1 : j == 2 ? w2 : w3;
        u32 wv512 = 0;
        if (lane < 32) wv512 = *(const __attribute__((address_space(1))) u16*)(wq + (r + 8 * (8 * c + (lane >> 2))) * 513 + 512);
        PIN();
        BAR();
        const u32 nb = __builtin_amdgcn_update_dpp(0u, wv512, 0xB1, 0xf, 0xf, false);
        if (lane < 32 && !(lane & 1))
            __builtin_amdgcn_raw_buffer_store_b64(u32x2{wv512 | (nb << 16), TAG}, rs, YOFF + r * 4096 + c * 128 + lane * 4, 0, SC1);
        BAR();
        BAR();
        if (c != 31) return;
        K2C k;
        {
            const float ru = (float)(r + 8 * n) * (1.0f / 2048.0f), e0 = (float)(4 * q), fn = (float)n;
            frag(e0 * 16.0f * ru, 16.0f * ru, k.F1r, k.F1i);
            frag(e0 * fn * 0.0625f, fn * 0.0625f, k.F2r, k.F2i);
            frag(-e0 * fn * 0.0625f, -fn * 0.0625f, k.G1r, k.G1i);
            const float b0 = fn * (float)(r + 32 * q) * (1.0f / 2048.0f), db = fn * (1.0f / 256.0f);
            frag(-16.0f * b0, -16.0f * db, k.G2r, k.G2i);
#pragma unroll
            for (int i = 0; i < 4; ++i) {
                k.t1c[i] = cosr((e0 + i) * ru); k.t1s[i] = sinr((e0 + i) * ru);
                k.t2c[i] = cosr(b0 + i * db); k.t2s[i] = sinr(b0 + i * db);
            }
            KEEP(k.F1r); KEEP(k.F1i); KEEP(k.F2r); KEEP(k.F2i); KEEP(k.G1r); KEEP(k.G1i); KEEP(k.G2r); KEEP(k.G2i);
            KEEP(k.t1c); KEEP(k.t1s); KEEP(k.t2c); KEEP(k.t2s);
        }
        const int xo = XOFF + r * 2048 + lane * 32, yo = YOFF + r * 4096 + (n + 64 * q) * 16;
        u32x4 xa[2], ya[4];
        for (unsigned spins = 0;;) {
            __builtin_amdgcn_s_sleep(8);
#pragma unroll
            for (int e = 0; e < 2; ++e) xa[e] = __builtin_amdgcn_raw_buffer_load_b128(rs, xo + e * 16, 0, SC1);
#pragma unroll
            for (int e = 0; e < 4; ++e) ya[e] = __builtin_amdgcn_raw_buffer_load_b128(rs, yo + e * 256, 0, SC1);
            u32 bad = 0;
#pragma unroll
            for (int e = 0; e < 2; ++e) bad |= (xa[e][1] ^ TAG) | (xa[e][3] ^ TAG);
#pragma unroll
            for (int e = 0; e < 4; ++e) bad |= (ya[e][1] ^ TAG) | (ya[e][3] ^ TAG);
            if (__all(bad == 0) || ++spins > SPIN_CAP) break;
        }
        BAR();
        const u32x4 f = {xa[0][0], xa[0][2], xa[1][0], xa[1][2]};
        const f32x4 er = mma(f, k.F1r, z4), ei = mma(f, k.F1i, z4);
        const u32x4 b = pk4(er * k.t1c + ei * k.t1s, ei * k.t1c - er * k.t1s);
        const f32x4 Xr = mma(k.F2r, b, z4), Xi = mma(k.F2i, b, z4);
        const u32x4 wa = {ya[0][0], ya[1][0], ya[2][0], ya[3][0]}, wb = {ya[0][2], ya[1][2], ya[2][2], ya[3][2]};
        const u32x4 oa = k2_inv(k, Xr, Xi, lo4(wa), hi4(wa)), ob = k2_inv(k, Xr, Xi, lo4(wb), hi4(wb));
        __builtin_amdgcn_raw_buffer_store_b128(u32x4{oa[0], ob[0], oa[1], ob[1]}, rs, xo, 0, SC1);
        __builtin_amdgcn_raw_buffer_store_b128(u32x4{oa[2], ob[2], oa[3], ob[3]}, rs, xo + 16, 0, SC1);
        BAR();
#pragma unroll
        for (int e = 0; e < 4; ++e) __builtin_amdgcn_raw_buffer_store_b128(u32x4{0u, 0u, 0u, 0u}, rs, yo + e * 256, 0, SC1);
        return;
    }
    {
        const u16* r0 = x + (size_t)(s1 + 256 * wv) * 1024;
        const u32 l0 = (u32)(size_t)(sm + L_XS) + wv * 2048;
        u32 keep;
        asm volatile("s_mov_b32 %0, m0\n\ts_mov_b32 m0, %3\n\ts_nop 0\n\t"
                     "global_load_lds_dwordx4 %1, %2\n\tglobal_load_lds_dwordx4 %1, %2 offset:1024\n\t"
                     "s_mov_b32 m0, %0"
                     : "=&s"(keep) : "v"(gvo), "s"(r0), "s"(l0) : "memory");
    }
    PIN();
    const __amdgpu_buffer_rsrc_t rg = __builtin_amdgcn_make_buffer_rsrc((void*)gw, 0, 2048, 0x00020000);
    const u32x4 gw0 = __builtin_amdgcn_raw_buffer_load_b128(rg, gvo, 0, 0);
    const u32x4 gw1 = __builtin_amdgcn_raw_buffer_load_b128(rg, gvo + 1024, 0, 0);
    PIN();
    const int xpos = XOFF + (((s1 & 15) + 16 * (s1 >> 6)) * 4 + ((s1 >> 4) & 3)) * 8;
    const int rho = 16 * h + n;
    const u32 mn = (n & 1) ? NEG2 : 0u;

    __amdgpu_buffer_rsrc_t rw[4];
    rw[0] = __builtin_amdgcn_make_buffer_rsrc((void*)w0, 0, NOW ? 0 : 2048 * 513 * 2, 0x00020000);
    rw[1] = __builtin_amdgcn_make_buffer_rsrc((void*)w1, 0, NOW ? 0 : 2048 * 513 * 2, 0x00020000);
    rw[2] = __builtin_amdgcn_make_buffer_rsrc((void*)w2, 0, NOW ? 0 : 2048 * 513 * 2, 0x00020000);
    rw[3] = __builtin_amdgcn_make_buffer_rsrc((void*)w3, 0, NOW ? 0 : 2048 * 513 * 2, 0x00020000);
    int wvo = ((r + 8 * (2 * wv + 16 * (lane >> 2))) * 513 + 16 * c + 4 * (lane & 3)) * 2;
    KEEP(wvo);
    u32x2 wl[8];
    f32x4 W[8];
#define WLOADS(lo, hi) do { PIN(); \
        _Pragma("unroll") for (int e = lo; e < hi; ++e) \
            wl[e] = __builtin_amdgcn_raw_buffer_load_b64(rw[e >> 1], wvo, (e & 1) * 8 * 513 * 2, 0); \
        PIN(); } while (0)
#define WSTAGE(lo, hi) do { \
        LDSP char* wt_ = sm + L_WT + wv * 4096 + lane * 8; \
        _Pragma("unroll") for (int e = lo; e < hi; ++e) *(LDSP u32x2*)(wt_ + e * 512) = wl[e]; \
        asm volatile("" ::: "memory"); \
        _Pragma("unroll") for (int e = lo; e < hi; ++e) wl[e] = trd(wt_ + e * 512); \
        PIN(); \
        _Pragma("unroll") for (int e = lo; e < hi; ++e) { \
            W[e] = f32x4{lo16(wl[e][0]), hi16(wl[e][0]), lo16(wl[e][1]), hi16(wl[e][1])}; KEEP(W[e]); } \
        } while (0)
    WLOADS(0, WE0);
    f32x4 tc[2], ts[2];
#pragma unroll
    for (int T = 0; T < 2; ++T)
#pragma unroll
        for (int i = 0; i < 4; ++i) {
            const float a = (float)((16 * T + 4 * q + i) * rho) * (1.0f / 1024.0f);
            tc[T][i] = cosr(a); ts[T][i] = sinr(a);
        }
    int lo1 = s1 * ROW + (2 * q) * 4096 + (rho + 128 * p) * 8;
    int zdo = L_ZS + q * ZP + rho * 128 + ((p ^ (rho & 7)) << 4);
    int zmo = L_ZS + q * ZP + (32 - rho) * 128 + (((7 - p) ^ ((32 - rho) & 7)) << 4);
    {
        const u32 c01 = pk(c4[0], c4[1]), c23 = pk(c4[2], c4[3]), s01 = pk(s4[0], s4[1]), s23 = pk(s4[2], s4[3]);
        const u32 mh = h ? 0x80000000u : 0u;
        u32x4 Cf = {c01 ^ mh, c23 ^ mh, c01 ^ mh ^ mn, c23 ^ mh ^ mn};
        u32x4 Sf = {s01 ^ mh, s23 ^ mh, s01 ^ mh ^ mn, s23 ^ mh ^ mn};
        u32x4 Nf = Sf ^ NEG2;
        u32x4 Are[2][2], Aim[2][2];
#pragma unroll
        for (int Tp = 0; Tp < 2; ++Tp)
#pragma unroll
            for (int T = 0; T < 2; ++T) {
                const u32 m = (Tp ? 0x80000000u : 0u) ^ (T ? mn : 0u);
                Are[Tp][T] = u32x4{c01 ^ m, c23 ^ m, s01 ^ m, s23 ^ m};
                Aim[Tp][T] = u32x4{s01 ^ m ^ NEG2, s23 ^ m ^ NEG2, c01 ^ m, c23 ^ m};
                KEEP(Are[Tp][T]); KEEP(Aim[Tp][T]);
            }
        u32x2 Sep;
        {
            const int rr = n >> 1, part = n & 1, sg = (rr & 1) ? -1 : 1;
            const float aa = (float)(q * rr + sg) * 0.125f, ab = (float)(q * rr - sg) * 0.125f;
            const float R2 = 1.41421356237f;
            const float ar = R2 * cosr(aa), ai = -R2 * sinr(aa), br = R2 * cosr(ab), bi = -R2 * sinr(ab);
            Sep[0] = part ? pk(ai, ar) : pk(ar, -ai);
            Sep[1] = part ? pk(bi, -br) : pk(br, bi);
        }
        KEEP(Cf); KEEP(Sf); KEEP(Nf); KEEP(Sep); KEEP(tc[0]); KEEP(tc[1]); KEEP(ts[0]); KEEP(ts[1]);
        const LDSP char* xs = sm + L_XS + p * 2048;
        const LDSP char* xb0 = xs + 64 * (4 * q + (n >> 2)) + 32 * (q & 1) + 8 * (n & 3);
        const LDSP char* xb1 = xs + 64 * (4 * q + (n >> 2)) + 32 * ((q & 1) ^ 1) + 8 * (n & 3);
        LDSP char* zw = sm + L_ZS + p * ZP + rho * 128;
        u32x2 g1[8];
#pragma unroll
        for (int e = 0; e < 8; ++e) { g1[e] = u32x2{0u, TAG}; KEEP(g1[e]); }
        KEEP(lo1); KEEP(zdo); KEEP(zmo);
        PIN();
        WLOADS(WE0, WE1);
        asm volatile("s_waitcnt vmcnt(%0)\n\ts_barrier" :: "n"(2 + WE1) : "memory");
        PIN();
        WLOADS(WE1, WE2);
        u32x4 Bd[2];
#pragma unroll
        for (int T = 0; T < 2; ++T) {
            const LDSP char* b = T ? xb1 : xb0;
            const u32x2 a0 = trd(b), a1 = trd(b + 1024), a2 = trd(b + 8192), a3 = trd(b + 9216);
            const u32x4 xr = {a0[0], a0[1], a1[0], a1[1]}, xi = {a2[0], a2[1], a3[0], a3[1]};
            f32x4 Dr = mma(xr, Cf, z4); Dr = mma(xi, Sf, Dr);
            f32x4 Di = mma(xr, Nf, z4); Di = mma(xi, Cf, Di);
            const f32x4 re = Dr * tc[T] + Di * ts[T], im = Di * tc[T] - Dr * ts[T];
            Bd[T] = u32x4{pk(re[0], re[1]), pk(re[2], re[3]), pk(im[0], im[1]), pk(im[2], im[3])};
        }
#pragma unroll
        for (int Tp = 0; Tp < 2; ++Tp) {
            f32x4 Zr = mma(Are[Tp][0], Bd[0], z4); Zr = mma(Are[Tp][1], Bd[1], Zr);
            f32x4 Zi = mma(Aim[Tp][0], Bd[0], z4); Zi = mma(Aim[Tp][1], Bd[1], Zi);
            const u32x4 o = pk4(Zr, Zi);
            *(LDSP u32x4*)(zw + (((4 * Tp + q) ^ (rho & 7)) << 4)) = o;
            if (rho == 0) {
                if (Tp == 1) {
#pragma unroll
                    for (int i = 0; i < 4; ++i) *(LDSP u32*)(zw + 32 * 128 + ((15 + 4 * q + i) << 2)) = o[i];
                } else if (q == 0) {
                    *(LDSP u32*)(zw + 32 * 128 + 31 * 4) = o[0];
                }
            }
        }
        BAR();
        {
            const u32x4 dv = *(const LDSP u32x4*)(sm + zdo);
            const u32x4 mv = *(const LDSP u32x4*)(sm + zmo);
            f32x4 d[4];
#pragma unroll
            for (int e = 0; e < 4; ++e) d[e] = mma16(Sep, u32x2{dv[e], mv[3 - e]}, z4);
            PIN();
#pragma unroll
            for (int e = 0; e < 8; ++e) {
                g1[e][0] = pk(d[e >> 1][2 * (e & 1)], d[e >> 1][2 * (e & 1) + 1]);
                __builtin_amdgcn_raw_buffer_store_b64(g1[e], rs, lo1 + (e >> 1) * 256, (e & 1) * 4096, SC1);
            }
            if (wv == 6) {
                const u32 v = *(const LDSP u32*)(sm + L_ZS + q * ZP + 64);
                const f32x4 dx = mma16(Sep, u32x2{v, v}, z4);
                if (n < 2) __builtin_amdgcn_raw_buffer_store_b64(u32x2{n ? pk(dx[2], dx[3]) : pk(dx[0], dx[1]), TAG}, rs, xpos + (2 * q + n) * 2048, 0, SC1);
            }
        }
        PIN();
    }
    u32x4 M1r[2], M1i[2], M2r[2], M2i[2], F2r, F2i, G1r, G1i;
    {
        const float ru = (float)(r + 8 * n) * (1.0f / 2048.0f), fn = (float)n;
#pragma unroll
        for (int aa = 0; aa < 2; ++aa) {
            frag((float)(2 * wv + aa + 64 * q) * ru, 16.0f * ru, M1r[aa], M1i[aa]);
            KEEP(M1r[aa]); KEEP(M1i[aa]);
        }
        frag((float)(4 * q) * fn * 0.0625f, fn * 0.0625f, F2r, F2i);
        G1r = F2r ^ 0x80000000u; G1i = F2i ^ 0x00008000u;
        KEEP(F2r); KEEP(F2i); KEEP(G1r); KEEP(G1i);
    }
    int lo2 = (2 * wv + 64 * q) * ROW + (r * 32 + c) * 128 + n * 8;
    KEEP(lo2);
    asm volatile("s_barrier" ::: "memory");
    WLOADS(WE2, WH1);
    asm volatile("s_waitcnt vmcnt(%0)" :: "n"(WH1 - WE2 + DR1) : "memory");
    PIN();
    {
        u32x2 a[8];
#pragma unroll
        for (int e = 0; e < 8; ++e) a[e] = __builtin_amdgcn_raw_buffer_load_b64(rs, lo2, ((e >> 2) + 16 * (e & 3)) * ROW, SC1);
        PIN();
        WLOADS(WH1, 8);
#pragma unroll
        for (int aa = 0; aa < 2; ++aa) {
            const float sa = (float)(2 * wv + aa + 16 * n) * (-1.0f / 2048.0f);
            frag(sa * (float)(r + 32 * q), sa * 8.0f, M2r[aa], M2i[aa]);
            KEEP(M2r[aa]); KEEP(M2i[aa]);
        }
        {
            const LDSP char* xs = sm + L_XS + wv * 2048 + lane * 16;
            const u32x4 x0 = *(const LDSP u32x4*)(xs), x1 = *(const LDSP u32x4*)(xs + 1024);
            float acc = 0.f, acc2 = 0.f;
#pragma unroll
            for (int i = 0; i < 4; ++i) { acc = dot2(x0[i], gw0[i], acc); acc2 = dot2(x1[i], gw1[i], acc2); }
            acc += acc2;
            acc = dpp_add<0x111, 0xf>(acc); acc = dpp_add<0x112, 0xf>(acc); acc = dpp_add<0x114, 0xf>(acc); acc = dpp_add<0x118, 0xf>(acc);
            acc = dpp_add<0x142, 0xa>(acc); acc = dpp_add<0x143, 0xc>(acc);
            const float z = bfr(acc);
            const float g = bfr(__builtin_amdgcn_rcpf(1.0f + __expf(-z)));
            const float og = bfr(1.0f - g);
            if (lane == 63) {
                LDSP float* G = (LDSP float*)(sm + L_G) + (wv & 3) * 4 + (wv >> 2);
                G[0] = g; G[2] = og;
            }
        }
        WSTAGE(0, WH1);
        int xw = 4 * q * 1024 + p * 256 + n * 16 + h * 8;
        KEEP(xw);
        PIN();
        u32 bad = 0;
#pragma unroll
        for (int e = 0; e < 8; ++e) bad |= a[e][1] ^ TAG;
        if (!__all(bad == 0)) {
            for (unsigned spins = 0; spins < SPIN_CAP; ++spins) {
                if (bad) {
#pragma unroll
                    for (int e = 0; e < 8; ++e) a[e] = __builtin_amdgcn_raw_buffer_load_b64(rs, lo2, ((e >> 2) + 16 * (e & 3)) * ROW, SC1);
                }
                bad = 0;
#pragma unroll
                for (int e = 0; e < 8; ++e) bad |= a[e][1] ^ TAG;
                if (__all(bad == 0)) break;
            }
        }
        u32x4 d[2];
#pragma unroll
        for (int aa = 0; aa < 2; ++aa) {
            const u32x4 f = {a[4 * aa][0], a[4 * aa + 1][0], a[4 * aa + 2][0], a[4 * aa + 3][0]};
            d[aa] = pk4(mma(M1r[aa], f, z4), mma(M1i[aa], f, z4));
        }
#pragma unroll
        for (int i = 0; i < 4; ++i) *(LDSP u32x2*)(sm + L_X1 + xw + i * 1024) = u32x2{d[0][i], d[1][i]};
        PIN();
        WSTAGE(WH1, 8);
        BAR();
        f32x4 Xr[2], Xi[2];
#pragma unroll
        for (int uu = 0; uu < 2; ++uu) {
            const u32x4 f = *(const LDSP u32x4*)(sm + L_X1 + (2 * wv + uu) * 1024 + lane * 16);
            Xr[uu] = mma(F2r, f, z4); Xi[uu] = mma(F2i, f, z4);
        }
#pragma unroll
        for (int f = 0; f < 2; ++f) {
            u32x4 ee[2];
#pragma unroll
            for (int uu = 0; uu < 2; ++uu) {
                const f32x4 wr = W[4 * f + uu], wi = W[4 * f + 2 + uu];
                const u32x4 y = pk4(Xr[uu] * wr - Xi[uu] * wi, Xr[uu] * wi + Xi[uu] * wr);
                ee[uu] = pk4(mma(G1r, y, z4), mma(G1i, y, z4));
            }
#pragma unroll
            for (int i = 0; i < 4; ++i) *(LDSP u32x2*)(sm + L_X2 + f * 16384 + xw + i * 1024) = u32x2{ee[0][i], ee[1][i]};
        }
        BAR();
#pragma unroll
        for (int aa = 0; aa < 2; ++aa) {
            const u32x4 fa = *(const LDSP u32x4*)(sm + L_X2 + (2 * wv + aa) * 1024 + lane * 16);
            const u32x4 fb = *(const LDSP u32x4*)(sm + L_X2 + 16384 + (2 * wv + aa) * 1024 + lane * 16);
            const u32x4 oa = pk4(mma(M2r[aa], fa, z4), mma(M2i[aa], fa, z4));
            const u32x4 ob = pk4(mma(M2r[aa], fb, z4), mma(M2i[aa], fb, z4));
#pragma unroll
            for (int i = 0; i < 4; ++i)
                __builtin_amdgcn_raw_buffer_store_b64(u32x2{oa[i], ob[i]}, rs, lo2, (aa + 16 * i) * ROW, SC1);
        }
    }
    u32x4 Fr[2][2], Fi[2][2], Er_[2], Ei_[2], Mix;
    {
        u32 fr[4], fi[4];
#pragma unroll
        for (int i = 0; i < 4; ++i) { fr[i] = pk(c4[i], -s4[i]); fi[i] = pk(s4[i], c4[i]); }
#pragma unroll
        for (int Tp = 0; Tp < 2; ++Tp)
#pragma unroll
            for (int T = 0; T < 2; ++T) {
                const u32 m0 = T ? mn : 0u, m1 = m0 ^ (Tp ? NEG2 : 0u);
                Fr[Tp][T] = u32x4{fr[0] ^ m0, fr[1] ^ m1, fr[2] ^ m0, fr[3] ^ m1};
                Fi[Tp][T] = u32x4{fi[0] ^ m0, fi[1] ^ m1, fi[2] ^ m0, fi[3] ^ m1};
                KEEP(Fr[Tp][T]); KEEP(Fi[Tp][T]);
            }
#pragma unroll
        for (int s = 0; s < 2; ++s) {
            const u32 m0 = s ? mn : 0u, m1 = m0 ^ (h ? NEG2 : 0u);
            Er_[s] = u32x4{fr[0] ^ m0, fr[1] ^ m1, fr[2] ^ m0, fr[3] ^ m1};
            Ei_[s] = u32x4{fi[0] ^ m0, fi[1] ^ m1, fi[2] ^ m0, fi[3] ^ m1};
            KEEP(Er_[s]); KEEP(Ei_[s]);
        }
        const f32x4 G = *(const LDSP f32x4*)(sm + L_G + (n >> 2) * 16);
        const int pp = n >> 2, i = n & 3;
        const float sc = 1.0f / 4194304.0f;
#pragma unroll
        for (int rr = 0; rr < 2; ++rr) {
            const float a = (float)(pp * (2 * q + rr)) * 0.125f;
            const float cc = cosr(a) * sc, ss = sinr(a) * sc;
            const float tau = ((rr == 0) == (i < 2)) ? 1.f : -1.f;
#pragma unroll
            for (int f = 0; f < 2; ++f) {
                const float g0 = G[2 * f], g1 = tau * G[2 * f + 1];
                const float cr = cc * g0 - ss * g1, ci = ss * g0 + cc * g1;
                Mix[2 * rr + f] = (i & 1) ? (i == 1 ? pk(ci, cr) : pk(-ci, -cr)) : pk(cr, -ci);
            }
        }
        KEEP(Mix);
    }
    PIN();
    asm volatile("s_waitcnt vmcnt(%0)" :: "n"(DR2) : "memory");
    PIN();
    {
        const int vx = xpos + q * 4096;
        u32x2 a[8], b0 = {0u, 0u}, b1 = {0u, 0u};
        bool ok = false;
        for (unsigned spins = 0;;) {
            if (!ok) {
#pragma unroll
                for (int e = 0; e < 8; ++e) a[e] = __builtin_amdgcn_raw_buffer_load_b64(rs, lo1 + (e >> 1) * 256, (e & 1) * 4096, SC1);
                if (wv == 6) {
                    b0 = __builtin_amdgcn_raw_buffer_load_b64(rs, vx, 0, SC1);
                    b1 = __builtin_amdgcn_raw_buffer_load_b64(rs, vx + 2048, 0, SC1);
                }
            }
            ok = b0[1] != TAG && b1[1] != TAG;
#pragma unroll
            for (int e = 0; e < 8; ++e) ok = ok & (a[e][1] != TAG);
            if (__all(ok) || ++spins > SPIN_CAP) break;
        }
        LDSP char* zq = sm + L_ZS + q * ZP;
        u32x4 dv, mv;
#pragma unroll
        for (int e = 0; e < 4; ++e) {
            f32x4 d = mma(Mix, u32x4{a[2 * e][0], a[2 * e][1], a[2 * e + 1][0], a[2 * e + 1][1]}, z4);
            if (e == 0 && wv == 0 && n == 0) { d[0] = 0.5f * (d[0] + d[2]); d[1] = 0.5f * (d[1] + d[3]); }
            dv[e] = pk(d[0], d[1]);
            mv[3 - e] = pk(d[2], d[3]);
        }
        *(LDSP u32x4*)(sm + zdo) = dv;
        *(LDSP u32x4*)(sm + zmo) = mv;
        if (rho == 0) {
#pragma unroll
            for (int e = 0; e < 4; ++e) {
                const int kk = 32 - 4 * p - e;
                if (kk < 32) *(LDSP u32*)(zq + kk * 4) = mv[3 - e];
            }
        }
        if (wv == 6) {
            const f32x4 d = mma(Mix, u32x4{b0[0], b0[1], b1[0], b1[1]}, z4);
            if (n == 0) *(LDSP u32*)(zq + 64) = pk(0.5f * (d[0] + d[2]), 0.5f * (d[1] + d[3]));
        }
    }
    BAR();
    {
        const LDSP char* zp = sm + L_ZS + p * ZP + n * 128;
        u32x4 Ad[2];
#pragma unroll
        for (int T = 0; T < 2; ++T) {
            const u32x4 a0 = *(const LDSP u32x4*)(zp + T * 2048 + ((q ^ (n & 7)) << 4));
            const u32x4 a1 = *(const LDSP u32x4*)(zp + T * 2048 + (((4 + q) ^ (n & 7)) << 4));
            f32x4 er = mma(a0, Er_[0], z4); er = mma(a1, Er_[1], er);
            f32x4 ei = mma(a0, Ei_[0], z4); ei = mma(a1, Ei_[1], ei);
            Ad[T] = pk4(er * tc[T] - ei * ts[T], ei * tc[T] + er * ts[T]);
        }
        const __amdgpu_buffer_rsrc_t ro = __builtin_amdgcn_make_buffer_rsrc(out, 0, 2048 * 1024 * 2, 0x00020000);
        const int vo = ((s1 + 256 * p) * 1024 + 16 * h + 4 * q + 32 * n) * 2;
#pragma unroll
        for (int Tp = 0; Tp < 2; ++Tp) {
            f32x4 orr = mma(Ad[0], Fr[Tp][0], z4); orr = mma(Ad[1], Fr[Tp][1], orr);
            f32x4 oi = mma(Ad[0], Fi[Tp][0], z4); oi = mma(Ad[1], Fi[Tp][1], oi);
            __builtin_amdgcn_raw_buffer_store_b64(u32x2{pk(orr[0], orr[1]), pk(orr[2], orr[3])}, ro, vo + 1024 * Tp, 0, 0);
            __builtin_amdgcn_raw_buffer_store_b64(u32x2{pk(oi[0], oi[1]), pk(oi[2], oi[3])}, ro, vo + 1024 * Tp, 2 << 20, 0);
        }
    }
}

extern "C" void kernel_launch(void* const* d_in, const int* in_sizes, int n_in,
                              void* d_out, int out_size, void* d_ws, size_t ws_size,
                              hipStream_t stream) {
    gf_fused<<<256, 576, 0, stream>>>((const u16*)d_in[0], (const u16*)d_in[6], (const u16*)d_in[1], (const u16*)d_in[2],
                                      (const u16*)d_in[4], (const u16*)d_in[5], (char*)d_ws, (u16*)d_out);
}
```

```cpp
#include <hip/hip_runtime.h>
#include <hip/hip_bf16.h>
#include <stdint.h>

typedef unsigned short u16;
typedef unsigned int u32;
typedef unsigned long long u64;
typedef __attribute__((ext_vector_type(8))) __bf16 bf16x8;
typedef __attribute__((ext_vector_type(2))) __bf16 bf16x2;
typedef __attribute__((ext_vector_type(4))) short s16x4;
typedef __attribute__((ext_vector_type(4))) float f32x4;
typedef __attribute__((ext_vector_type(2))) float f32x2;
typedef __attribute__((ext_vector_type(4))) unsigned u32x4;
typedef __attribute__((ext_vector_type(2))) unsigned u32x2;
#define LDSP __attribute__((address_space(3)))
#define DI __device__ __forceinline__

DI u32 pk(float lo, float hi) {
    f32x2 t = {lo, hi};
    return __builtin_bit_cast(u32, __builtin_convertvector(t, bf16x2));
}
DI u32x4 pk4(f32x4 a, f32x4 b) { return u32x4{pk(a[0], b[0]), pk(a[1], b[1]), pk(a[2], b[2]), pk(a[3], b[3])}; }
DI float bfr(float v) { return (float)(__bf16)v; }
DI f32x4 mma(u32x4 a, u32x4 b, f32x4 c) {
    return __builtin_amdgcn_mfma_f32_16x16x32_bf16(__builtin_bit_cast(bf16x8, a), __builtin_bit_cast(bf16x8, b), c, 0, 0, 0);
}
DI f32x4 mma16(u32x2 a, u32x2 b, f32x4 c) {
    return __builtin_amdgcn_mfma_f32_16x16x16bf16_1k(__builtin_bit_cast(s16x4, a), __builtin_bit_cast(s16x4, b), c, 0, 0, 0);
}
DI float dot2(u32 a, u32 b, float c) {
    return __builtin_amdgcn_fdot2_f32_bf16(__builtin_bit_cast(bf16x2, a), __builtin_bit_cast(bf16x2, b), c, false);
}
DI float cosr(float x) { return __builtin_amdgcn_cosf(x); }
DI float sinr(float x) { return __builtin_amdgcn_sinf(x); }
DI float lo16(u32 v) { return __builtin_bit_cast(float, v << 16); }
DI float hi16(u32 v) { return __builtin_bit_cast(float, v & 0xffff0000u); }
DI f32x4 lo4(u32x4 v) { return f32x4{lo16(v[0]), lo16(v[1]), lo16(v[2]), lo16(v[3])}; }
DI f32x4 hi4(u32x4 v) { return f32x4{hi16(v[0]), hi16(v[1]), hi16(v[2]), hi16(v[3])}; }
DI u32x2 trd(const LDSP char* p) {
    return __builtin_bit_cast(u32x2, __builtin_amdgcn_ds_read_tr16_b64_v4i16((LDSP s16x4*)p));
}
template <int CTRL, int RMASK> DI float dpp_add(float v) {
    const int t = __builtin_amdgcn_update_dpp(0, __builtin_bit_cast(int, v), CTRL, RMASK, 0xf, false);
    return v + __builtin_bit_cast(float, t);
}
#define BAR() asm volatile("s_waitcnt lgkmcnt(0)\n\ts_barrier" ::: "memory")
#define PIN() __builtin_amdgcn_sched_barrier(0)
#define KEEP(x) asm volatile("" : "+v"(x))
#define REREAD() asm volatile("" ::: "memory")

static constexpr u32 TAG = 0xFFAB7F95u;
static constexpr int SC1 = 16;
static constexpr int XOFF = 256 * 65536;
static constexpr int YOFF = XOFF + 8 * 2048;
static constexpr int DOFF = YOFF + 8 * 4096;
static constexpr int WS_BYTES = DOFF + 2048;
DI int lineoff(int j) { return (j >> 6) * 16384 + (j & 63) * 128; }
static constexpr int ZP = 33 * 128;
static constexpr int L_XS = 0;
static constexpr int L_ZS = 16384;
static constexpr int L_X1 = L_ZS + 4 * ZP;
static constexpr int L_X2 = L_X1 + 16384;
static constexpr int L_WT = L_X2 + 32768;
static constexpr int L_G = L_WT + 32768;
static constexpr int L_H = L_G + 64;
static constexpr int L_END = L_H + 16;
static constexpr unsigned SPIN_CAP = 1u << 16;
static constexpr u32 NEG2 = 0x80008000u;

template <int SG> DI void cmul(f32x4 ar, f32x4 ai, f32x4 c, f32x4 s, f32x4& re, f32x4& im) {
#pragma unroll
    for (int i = 0; i < 4; ++i) {
        float t0 = ar[i] * c[i], t1 = ai[i] * c[i];
        asm("" : "+v"(t0)); asm("" : "+v"(t1));
        t0 = __builtin_fmaf(SG > 0 ? -ai[i] : ai[i], s[i], t0); t1 = __builtin_fmaf(SG > 0 ? ar[i] : -ar[i], s[i], t1);
        asm("" : "+v"(t0)); asm("" : "+v"(t1));
        re[i] = t0; im[i] = t1;
    }
}

DI u32 now32() { u64 t; asm volatile("s_memrealtime %0\n\ts_waitcnt lgkmcnt(0)" : "=s"(t) :: "memory"); return (u32)t; }
DI void hold_back(u32 stamp, int delta) {
    const u32 due = (stamp + delta) << 1;
    for (int i = 0; i < 128; ++i)
        if (!__any((int)(due - (now32() << 1)) > 0)) break;
}

struct K2C {
    u32x4 F1r, F1i, F2r, F2i, G1r, G1i, G2r, G2i;
    f32x4 t1c, t1s, t2c, t2s;
};
DI void frag(float a0, float da, u32x4& fr, u32x4& fi, float sc = 1.0f) {
#pragma unroll
    for (int i = 0; i < 4; ++i) {
        const float a = a0 + (float)i * da, cc = sc * cosr(a), ss = sc * sinr(a);
        fr[i] = pk(cc, ss); fi[i] = pk(-ss, cc);
    }
}
DI u32x4 k2_inv(const K2C& k, f32x4 Xr, f32x4 Xi, f32x4 wr, f32x4 wi) {
    const f32x4 z4 = {0.f, 0.f, 0.f, 0.f};
    const u32x4 y = pk4(Xr * wr - Xi * wi, Xr * wi + Xi * wr);
    const f32x4 er = mma(y, k.G1r, z4), ei = mma(y, k.G1i, z4);
    const u32x4 b = pk4(er * k.t2c - ei * k.t2s, ei * k.t2c + er * k.t2s);
    return pk4(mma(k.G2r, b, z4), mma(k.G2i, b, z4));
}

typedef __attribute__((ext_vector_type(8))) unsigned long long u64x8;

__global__ __launch_bounds__(576, 3) void gf_fused(const u16* x_, const u16* gw_, const u16* w0_, const u16* w1_,
                                                const u16* w2_, const u16* w3_, char* ws_, u16* out_) {
    __shared__ __attribute__((aligned(16))) char smem[L_END];
    LDSP char* sm = (LDSP char*)smem;
    const int tid = threadIdx.x, lane = tid & 63;
    const int wv = __builtin_amdgcn_readfirstlane(tid >> 6);
    const int id = blockIdx.x;
    {
        u64 pc; u32 t0, t1;
        asm volatile("s_cmp_lt_u32 %3, 8\n\ts_cbranch_scc1 .Lgf_nopf\n\t"
                     "s_getpc_b64 %0\n.Lgf_pc:\n\t"
                     "s_mov_b32 %1, .Lgf_help-.Lgf_pc\n\t"
                     "s_load_dword %2, %0, %1 offset:0x0\n\ts_load_dword %2, %0, %1 offset:0x80\n\t"
                     "s_load_dword %2, %0, %1 offset:0x100\n\ts_load_dword %2, %0, %1 offset:0x180\n\t"
                     "s_load_dword %2, %0, %1 offset:0x200\n\ts_load_dword %2, %0, %1 offset:0x280\n\t"
                     "s_min_u32 %1, %4, .Lgf_last-.Lgf_pc-384\n\t"
                     "s_load_dword %2, %0, %1 offset:0x0\n\ts_load_dword %2, %0, %1 offset:0x80\n\t"
                     "s_load_dword %2, %0, %1 offset:0x100\n\ts_load_dword %2, %0, %1 offset:0x180\n\t"
                     "s_waitcnt lgkmcnt(0)\n.Lgf_nopf:"
                     : "=&s"(pc), "=&s"(t0), "=&s"(t1) : "s"(wv), "s"((id >> 3) * 512) : "scc");
    }
    const int p = wv >> 1, h = wv & 1;
    const int q = lane >> 4, n = lane & 15;
    const int s1 = (id & 7) * 32 + (id >> 3);
    const int r = id & 7, c = id >> 3;
    const f32x4 z4 = {0.f, 0.f, 0.f, 0.f};
#define DPOS(s) (DOFF + (((((s) >> 1) & 7) * 4 + ((s) >> 6)) * 8 + ((s) & 1) * 4 + (((s) >> 4) & 3)) * 8)
#define KA_FETCH() u64x8 ka; asm volatile("s_load_dwordx16 %0, %1, 0x0" : "=s"(ka) : "s"(__builtin_amdgcn_kernarg_segment_ptr())); PIN()
#define KA_WAIT() PIN(); asm volatile("s_waitcnt lgkmcnt(0)" : "+s"(ka)); PIN(); \
    const u16* w0 = (const u16*)ka[2]; const u16* w1 = (const u16*)ka[3]; const u16* w2 = (const u16*)ka[4]; const u16* w3 = (const u16*)ka[5]; \
    char* ws = (char*)((ka[6] + 65535) & ~(u64)65535); \
    const __amdgpu_buffer_rsrc_t rs = __builtin_amdgcn_make_buffer_rsrc(ws, 0, WS_BYTES, 0x00020000)
    if (wv == 8) {
        asm volatile(".Lgf_help:");
        KA_FETCH();
        KA_WAIT();
        const int j = lane & 3;
        const u16* wq = j == 0 ? w0 : j == 1 ? w1 : j == 2 ? w2 : w3;
        const char* own = ws + (size_t)s1 * 65536;
        __builtin_amdgcn_s_setprio(3);
        const u32 po = ((s1 >> 6) << 14) + (s1 & 63) * 128;
        u64 ta, tb, tc_; u32 dm;
        asm volatile("s_load_dword %3, %4, %7\n\t"
                     "s_sleep 3\n\ts_waitcnt lgkmcnt(0)\n\t"
                     "s_memtime %0\n\ts_load_dword %3, %4, %5 glc\n\ts_waitcnt lgkmcnt(0)\n\t"
                     "s_memtime %1\n\ts_load_dword %3, %4, %6 glc\n\ts_waitcnt lgkmcnt(0)\n\t"
                     "s_memtime %2\n\ts_waitcnt lgkmcnt(0)"
                     : "=&s"(ta), "=&s"(tb), "=&s"(tc_), "=&s"(dm) : "s"(own), "s"(po), "s"(po ^ 0x6000u), "s"(po ^ 0x1000u) : "memory");
        const u32 tq0 = (u32)tb - (u32)ta, tq1 = (u32)tc_ - (u32)tb;
        const u32 hsel = tq0 > tq1 ? 0x2000u : 0u;
        const u32 tnow = now32();
        if (lane == 0) {
            *(LDSP u32x2*)(sm + L_H) = u32x2{hsel, 0u};
            __builtin_amdgcn_raw_buffer_store_b64(u32x2{(hsel << 18) | (tnow & 0x7fffffffu), TAG}, rs, DPOS(s1), 0, SC1);
        }
        u32 wv512 = 0;
        if (lane < 32) wv512 = *(const __attribute__((address_space(1))) u16*)(wq + (r + 8 * (8 * c + (lane >> 2))) * 513 + 512);
        PIN();
        BAR();
        BAR();
        BAR();
        const u32 nb = __builtin_amdgcn_update_dpp(0u, wv512, 0xB1, 0xf, 0xf, false);
        if (lane < 32 && !(lane & 1))
            __builtin_amdgcn_raw_buffer_store_b64(u32x2{wv512 | (nb << 16), TAG}, rs, YOFF + r * 4096 + c * 128 + lane * 4, 0, SC1);
        if (c != 31) { BAR(); return; }
        __builtin_amdgcn_s_setprio(0);
        K2C k;
        {
            const float ru = (float)(r + 8 * n) * (1.0f / 2048.0f), e0 = (float)(4 * q), fn = (float)n;
            frag(e0 * 16.0f * ru, 16.0f * ru, k.F1r, k.F1i);
            frag(e0 * fn * 0.0625f, fn * 0.0625f, k.F2r, k.F2i);
            frag(-e0 * fn * 0.0625f, -fn * 0.0625f, k.G1r, k.G1i);
            const float b0 = fn * (float)(r + 32 * q) * (1.0f / 2048.0f), db = fn * (1.0f / 256.0f);
            frag(-16.0f * b0, -16.0f * db, k.G2r, k.G2i, 1.0f / 256.0f);
#pragma unroll
            for (int i = 0; i < 4; ++i) {
                k.t1c[i] = cosr((e0 + i) * ru); k.t1s[i] = sinr((e0 + i) * ru);
                k.t2c[i] = cosr(b0 + i * db); k.t2s[i] = sinr(b0 + i * db);
            }
            KEEP(k.F1r); KEEP(k.F1i); KEEP(k.F2r); KEEP(k.F2i); KEEP(k.G1r); KEEP(k.G1i); KEEP(k.G2r); KEEP(k.G2i);
            KEEP(k.t1c); KEEP(k.t1s); KEEP(k.t2c); KEEP(k.t2s);
        }
        const int xo = XOFF + r * 2048 + lane * 32, yo = YOFF + r * 4096 + (n + 64 * q) * 16;
        u32x4 xa[2], ya[4];
#define XYLOAD() do { \
        _Pragma("unroll") for (int e = 0; e < 2; ++e) xa[e] = __builtin_amdgcn_raw_buffer_load_b128(rs, xo + e * 16, 0, SC1); \
        _Pragma("unroll") for (int e = 0; e < 4; ++e) ya[e] = __builtin_amdgcn_raw_buffer_load_b128(rs, yo + e * 256, 0, SC1); } while (0)
        for (int i = 0; i < (1 << 20); ++i) {
            if (__builtin_amdgcn_readfirstlane(*(const volatile LDSP u32*)(sm + L_H + 4))) break;
            __builtin_amdgcn_s_sleep(2);
        }
        XYLOAD();
        PIN();
        BAR();
        bool owed = true;
        for (unsigned spins = 0;;) {
            REREAD();
            u32 bad = 0;
#pragma unroll
            for (int e = 0; e < 2; ++e) bad |= (xa[e][1] ^ TAG) | (xa[e][3] ^ TAG);
#pragma unroll
            for (int e = 0; e < 4; ++e) bad |= (ya[e][1] ^ TAG) | (ya[e][3] ^ TAG);
            if (__all(bad == 0) || ++spins > SPIN_CAP) break;
            XYLOAD();
            PIN();
            if (owed) { BAR(); owed = false; }
        }
        const u32x4 f = {xa[0][0], xa[0][2], xa[1][0], xa[1][2]};
        const f32x4 er = mma(f, k.F1r, z4), ei = mma(f, k.F1i, z4);
        const u32x4 b = pk4(er * k.t1c + ei * k.t1s, ei * k.t1c - er * k.t1s);
        const f32x4 Xr = mma(k.F2r, b, z4), Xi = mma(k.F2i, b, z4);
        const u32x4 wa = {ya[0][0], ya[1][0], ya[2][0], ya[3][0]}, wb = {ya[0][2], ya[1][2], ya[2][2], ya[3][2]};
        const u32x4 oa = k2_inv(k, Xr, Xi, lo4(wa), hi4(wa)), ob = k2_inv(k, Xr, Xi, lo4(wb), hi4(wb));
        __builtin_amdgcn_raw_buffer_store_b128(u32x4{oa[0], ob[0], oa[1], ob[1]}, rs, xo, 0, SC1);
        __builtin_amdgcn_raw_buffer_store_b128(u32x4{oa[2], ob[2], oa[3], ob[3]}, rs, xo + 16, 0, SC1);
        if (owed) BAR();
#pragma unroll
        for (int e = 0; e < 4; ++e) __builtin_amdgcn_raw_buffer_store_b128(u32x4{0u, 0u, 0u, 0u}, rs, yo + e * 256, 0, SC1);
        asm volatile(".Lgf_last:");
        return;
    }
    KA_FETCH();
    float c4[4], s4[4];
#pragma unroll
    for (int i = 0; i < 4; ++i) {
        const float a = (float)((4 * q + i) * n) * (1.0f / 32.0f);
        c4[i] = cosr(a); s4[i] = sinr(a);
        KEEP(c4[i]); KEEP(s4[i]);
    }
    int gvo = (lane ^ (((lane >> 4) & 1) << 1)) * 16;
    KEEP(gvo);
    __builtin_amdgcn_s_setprio(1);
    KA_WAIT();
    const u16* x = (const u16*)ka[0];
    const u16* gw = (const u16*)ka[1];
    u16* out = (u16*)ka[7];
    {
        const u16* r0 = x + (size_t)(s1 + 256 * wv) * 1024;
        const u32 l0 = (u32)(size_t)(sm + L_XS) + wv * 2048;
        u32 keep;
        asm volatile("s_mov_b32 %0, m0\n\ts_mov_b32 m0, %3\n\ts_nop 0\n\t"
                     "global_load_lds_dwordx4 %1, %2\n\tglobal_load_lds_dwordx4 %1, %2 offset:1024\n\t"
                     "s_mov_b32 m0, %0"
                     : "=&s"(keep) : "v"(gvo), "s"(r0), "s"(l0) : "memory");
    }
    PIN();
    __builtin_amdgcn_raw_buffer_store_b64(u32x2{0u, 0u}, rs, (wv == 0 ? s1 * 65536 : 0x40000000) + lineoff((64 * q) ^ s1) + (q & 1) * 0x2000 + n * 8, 0, SC1);
    PIN();
    const __amdgpu_buffer_rsrc_t rg = __builtin_amdgcn_make_buffer_rsrc((void*)gw, 0, 2048, 0x00020000);
    const u32x4 gw0 = __builtin_amdgcn_raw_buffer_load_b128(rg, gvo, 0, 0);
    const u32x4 gw1 = __builtin_amdgcn_raw_buffer_load_b128(rg, gvo + 1024, 0, 0);
    PIN();
    const int xpos = XOFF + (((s1 & 15) + 16 * (s1 >> 6)) * 4 + ((s1 >> 4) & 3)) * 8;
    const int rho = 16 * h + n;
    const u32 mn = (n & 1) ? NEG2 : 0u;

    __amdgpu_buffer_rsrc_t rw[4];
    rw[0] = __builtin_amdgcn_make_buffer_rsrc((void*)w0, 0, 2048 * 513 * 2, 0x00020000);
    rw[1] = __builtin_amdgcn_make_buffer_rsrc((void*)w1, 0, 2048 * 513 * 2, 0x00020000);
    rw[2] = __builtin_amdgcn_make_buffer_rsrc((void*)w2, 0, 2048 * 513 * 2, 0x00020000);
    rw[3] = __builtin_amdgcn_make_buffer_rsrc((void*)w3, 0, 2048 * 513 * 2, 0x00020000);
    int wvo = ((r + 8 * (2 * wv + 16 * (lane >> 2))) * 513 + 16 * c + 4 * (lane & 3)) * 2;
    KEEP(wvo);
    u32x2 wl[8];
    f32x4 W[8];
#define WLOADS(lo, hi) do { PIN(); \
        _Pragma("unroll") for (int e = lo; e < hi; ++e) \
            wl[e] = __builtin_amdgcn_raw_buffer_load_b64(rw[e >> 1], wvo, (e & 1) * 8 * 513 * 2, 0); \
        PIN(); } while (0)
#define WSTAGE(lo, hi) do { \
        LDSP char* wt_ = sm + L_WT + wv * 4096 + lane * 8; \
        _Pragma("unroll") for (int e = lo; e < hi; ++e) *(LDSP u32x2*)(wt_ + e * 512) = wl[e]; \
        asm volatile("" ::: "memory"); \
        _Pragma("unroll") for (int e = lo; e < hi; ++e) wl[e] = trd(wt_ + e * 512); \
        PIN(); \
        _Pragma("unroll") for (int e = lo; e < hi; ++e) { \
            W[e] = f32x4{lo16(wl[e][0]), hi16(wl[e][0]), lo16(wl[e][1]), hi16(wl[e][1])}; KEEP(W[e]); } \
        } while (0)
    WLOADS(0, 2);
    f32x4 tc[2], ts[2];
#pragma unroll
    for (int T = 0; T < 2; ++T)
#pragma unroll
        for (int i = 0; i < 4; ++i) {
            const float a = (float)((16 * T + 4 * q + i) * rho) * (1.0f / 1024.0f);
            tc[T][i] = cosr(a); ts[T][i] = sinr(a);
        }
    int lo1 = s1 * 65536 + lineoff(((64 * q + 8 * p + h) ^ s1) & 0xd9) + n * 8;
#define SO1(e) ((((((e) & 1) * 32 + ((e) >> 1) * 2) ^ s1) & 0x26) * 128)
    int dro = DOFF + ((wv * 4 + q) * 8 + (n & 7)) * 8;
    KEEP(dro);
    u32x2 dir;
    int zdo = L_ZS + q * ZP + rho * 128 + ((p ^ (rho & 7)) << 4);
    int zmo = L_ZS + q * ZP + (32 - rho) * 128 + (((7 - p) ^ ((32 - rho) & 7)) << 4);
    {
        const u32 c01 = pk(c4[0], c4[1]), c23 = pk(c4[2], c4[3]), s01 = pk(s4[0], s4[1]), s23 = pk(s4[2], s4[3]);
        const u32 mh = h ? 0x80000000u : 0u;
        u32x4 Cf = {c01 ^ mh, c23 ^ mh, c01 ^ mh ^ mn, c23 ^ mh ^ mn};
        u32x4 Sf = {s01 ^ mh, s23 ^ mh, s01 ^ mh ^ mn, s23 ^ mh ^ mn};
        u32x4 Nf = Sf ^ NEG2;
        u32x4 Are[2][2], Aim[2][2];
#pragma unroll
        for (int Tp = 0; Tp < 2; ++Tp)
#pragma unroll
            for (int T = 0; T < 2; ++T) {
                const u32 m = (Tp ? 0x80000000u : 0u) ^ (T ? mn : 0u);
                Are[Tp][T] = u32x4{c01 ^ m, c23 ^ m, s01 ^ m, s23 ^ m};
                Aim[Tp][T] = u32x4{s01 ^ m ^ NEG2, s23 ^ m ^ NEG2, c01 ^ m, c23 ^ m};
                KEEP(Are[Tp][T]); KEEP(Aim[Tp][T]);
            }
        u32x2 Sep;
        {
            const int rr = n >> 1, part = n & 1, sg = (rr & 1) ? -1 : 1;
            const float aa = (float)(q * rr + sg) * 0.125f, ab = (float)(q * rr - sg) * 0.125f;
            const float R2 = 0.70710678119f;
            const float ar = R2 * cosr(aa), ai = -R2 * sinr(aa), br = R2 * cosr(ab), bi = -R2 * sinr(ab);
            Sep[0] = part ? pk(ai, ar) : pk(ar, -ai);
            Sep[1] = part ? pk(bi, -br) : pk(br, bi);
        }
        KEEP(Cf); KEEP(Sf); KEEP(Nf); KEEP(Sep); KEEP(tc[0]); KEEP(tc[1]); KEEP(ts[0]); KEEP(ts[1]);
        int xb0 = L_XS + p * 2048 + 64 * (4 * q + (n >> 2)) + 32 * (q & 1) + 8 * (n & 3), xb1 = xb0 ^ 32;
        int zw0 = L_ZS + p * ZP + rho * 128 + ((q ^ (rho & 7)) << 4), zw1 = zw0 ^ 64;
        KEEP(xb0); KEEP(xb1); KEEP(zw0); KEEP(zw1);
        LDSP char* zw = sm + L_ZS + p * ZP;
        u32x2 g1[8];
#pragma unroll
        for (int e = 0; e < 8; ++e) { g1[e] = u32x2{0u, TAG}; KEEP(g1[e]); }
        KEEP(lo1); KEEP(zdo); KEEP(zmo);
        PIN();
        WLOADS(2, 4);
        asm volatile("s_waitcnt vmcnt(7)\n\ts_barrier" ::: "memory");
        PIN();
        u32x4 xr[2], xi[2];
#pragma unroll
        for (int T = 0; T < 2; ++T) {
            const LDSP char* b = sm + (T ? xb1 : xb0);
            const u32x2 a0 = trd(b), a1 = trd(b + 1024), a2 = trd(b + 8192), a3 = trd(b + 9216);
            xr[T] = u32x4{a0[0], a0[1], a1[0], a1[1]}; xi[T] = u32x4{a2[0], a2[1], a3[0], a3[1]};
        }
        const u32 hs = *(const LDSP u32*)(sm + L_H);
        PIN();
        f32x4 Dr[2], Di[2];
#pragma unroll
        for (int T = 0; T < 2; ++T) {
            Dr[T] = mma(xr[T], Cf, z4); Di[T] = mma(xr[T], Nf, z4);
            Dr[T] = mma(xi[T], Sf, Dr[T]); Di[T] = mma(xi[T], Cf, Di[T]);
            PIN();
        }
        lo1 += hs;
        f32x4 Zr[2], Zi[2], re, im;
        cmul<-1>(Dr[0], Di[0], tc[0], ts[0], re, im);
        const u32x4 Bd0 = {pk(re[0], re[1]), pk(re[2], re[3]), pk(im[0], im[1]), pk(im[2], im[3])};
        PIN();
#pragma unroll
        for (int Tp = 0; Tp < 2; ++Tp) { Zr[Tp] = mma(Are[Tp][0], Bd0, z4); Zi[Tp] = mma(Aim[Tp][0], Bd0, z4); }
        cmul<-1>(Dr[1], Di[1], tc[1], ts[1], re, im);
        const u32x4 Bd1 = {pk(re[0], re[1]), pk(re[2], re[3]), pk(im[0], im[1]), pk(im[2], im[3])};
        PIN();
#pragma unroll
        for (int Tp = 0; Tp < 2; ++Tp) { Zr[Tp] = mma(Are[Tp][1], Bd1, Zr[Tp]); Zi[Tp] = mma(Aim[Tp][1], Bd1, Zi[Tp]); }
        PIN();
        const u32x4 o0 = pk4(Zr[0], Zi[0]);
        *(LDSP u32x4*)(sm + zw0) = o0;
        const u32x4 o1 = pk4(Zr[1], Zi[1]);
        *(LDSP u32x4*)(sm + zw1) = o1;
        if (rho == 0) {
#pragma unroll
            for (int i = 0; i < 4; ++i) *(LDSP u32*)(zw + 32 * 128 + ((15 + 4 * q + i) << 2)) = o1[i];
            *(LDSP u32*)(zw + 32 * 128 + (q == 0 ? 31 : 14) * 4) = o0[0];
        }
        BAR();
        {
            const u32x4 dv = *(const LDSP u32x4*)(sm + zdo);
            const u32x4 mv = *(const LDSP u32x4*)(sm + zmo);
            f32x4 d[4];
#pragma unroll
            for (int e = 0; e < 4; ++e) d[e] = mma16(Sep, u32x2{dv[e], mv[3 - e]}, z4);
            PIN();
#pragma unroll
            for (int e = 0; e < 8; ++e) {
                g1[e][0] = pk(d[e >> 1][2 * (e & 1)], d[e >> 1][2 * (e & 1) + 1]);
                __builtin_amdgcn_raw_buffer_store_b64(g1[e], rs, lo1, SO1(e), SC1);
            }
            if (wv == 6) {
                const u32 v = *(const LDSP u32*)(sm + L_ZS + q * ZP + 64);
                const f32x4 dx = mma16(Sep, u32x2{v, v}, z4);
                if (n < 2) __builtin_amdgcn_raw_buffer_store_b64(u32x2{n ? pk(dx[2], dx[3]) : pk(dx[0], dx[1]), TAG}, rs, xpos + (2 * q + n) * 2048, 0, SC1);
            }
        }
        PIN();
    }
    dir = __builtin_amdgcn_raw_buffer_load_b64(rs, dro, 0, SC1);
    PIN();
    u32x4 M1r[2], M1i[2], M2r[2], M2i[2], F2r, F2i, G1r, G1i;
    {
        const float ru = (float)(r + 8 * n) * (1.0f / 2048.0f), fn = (float)n;
#pragma unroll
        for (int aa = 0; aa < 2; ++aa) {
            frag((float)(2 * wv + aa + 64 * q) * ru, 16.0f * ru, M1r[aa], M1i[aa]);
            KEEP(M1r[aa]); KEEP(M1i[aa]);
        }
        frag((float)(4 * q) * fn * 0.0625f, fn * 0.0625f, F2r, F2i);
        G1r = F2r ^ 0x80000000u; G1i = F2i ^ 0x00008000u;
        KEEP(F2r); KEEP(F2i); KEEP(G1r); KEEP(G1i);
    }
    const int rc = r * 32 + c;
    int lo2b = (2 * wv + 64 * q) * 65536 + lineoff(((2 * wv + 64 * q) ^ rc) & 0xce) + n * 8;
#define SO2(e) ((((e) >> 2) + 16 * ((e) & 3)) * 65536 + (((((e) >> 2) + 16 * ((e) & 3)) ^ rc) & 0x31) * 128)
    KEEP(lo2b);
    int lo2[8];
    asm volatile("s_barrier" ::: "memory");
    PIN();
    if (!__all(dir[1] == TAG)) {
        for (unsigned spins = 0; spins < SPIN_CAP; ++spins) {
            REREAD();
            __builtin_amdgcn_s_sleep(4);
            dir = __builtin_amdgcn_raw_buffer_load_b64(rs, dro, 0, SC1);
            if (__all(dir[1] == TAG)) break;
        }
    }
    {
        const u64 hm = __ballot((int)dir[0] < 0);
        const u32 m = (u32)(hm >> (16 * q)) << 13;
#pragma unroll
        for (int e = 0; e < 8; ++e) { lo2[e] = lo2b | ((m >> e) & 0x2000u); KEEP(lo2[e]); }
        hold_back(dir[0], 140);
    }
    PIN();
    {
        u32x2 a[8];
#pragma unroll
        for (int e = 0; e < 8; ++e) a[e] = __builtin_amdgcn_raw_buffer_load_b64(rs, lo2[e], SO2(e), SC1);
        PIN();
        if (wv == 7) *(LDSP u32*)(sm + L_H + 4) = 1u;
        WLOADS(4, 8);
#pragma unroll
        for (int aa = 0; aa < 2; ++aa) {
            const float sa = (float)(2 * wv + aa + 16 * n) * (-1.0f / 2048.0f);
            frag(sa * (float)(r + 32 * q), sa * 8.0f, M2r[aa], M2i[aa], 1.0f / 256.0f);
            KEEP(M2r[aa]); KEEP(M2i[aa]);
        }
        {
            const LDSP char* xs = sm + L_XS + wv * 2048 + lane * 16;
            const u32x4 x0 = *(const LDSP u32x4*)(xs), x1 = *(const LDSP u32x4*)(xs + 1024);
            float acc = 0.f, acc2 = 0.f;
#pragma unroll
            for (int i = 0; i < 4; ++i) {
                acc = dot2(pk(lo16(x0[i]) * lo16(gw0[i]), hi16(x0[i]) * hi16(gw0[i])), 0x3f803f80u, acc);
                acc2 = dot2(pk(lo16(x1[i]) * lo16(gw1[i]), hi16(x1[i]) * hi16(gw1[i])), 0x3f803f80u, acc2);
            }
            acc += acc2;
            acc = dpp_add<0x111, 0xf>(acc); acc = dpp_add<0x112, 0xf>(acc); acc = dpp_add<0x114, 0xf>(acc); acc = dpp_add<0x118, 0xf>(acc);
            acc = dpp_add<0x142, 0xa>(acc); acc = dpp_add<0x143, 0xc>(acc);
            const float z = bfr(acc);
            const float g = bfr(__builtin_amdgcn_rcpf(1.0f + __expf(-z)));
            const float og = bfr(1.0f - g);
            if (lane == 63) {
                LDSP float* G = (LDSP float*)(sm + L_G) + (wv & 3) * 4 + (wv >> 2);
                G[0] = g; G[2] = og;
            }
        }
        WSTAGE(0, 4);
        int xw = 4 * q * 1024 + p * 256 + n * 16 + h * 8;
        int x2r = L_X2 + wv * 2048 + lane * 16;
        int xw2a = L_X2 + xw, xw2b = L_X2 + 16384 + xw;
        KEEP(xw); KEEP(x2r); KEEP(xw2a); KEEP(xw2b);
        PIN();
        u32 bad = 0;
#pragma unroll
        for (int e = 0; e < 8; ++e) bad |= a[e][1] ^ TAG;
        KEEP(bad);
        if (!__all(bad == 0)) {
            for (unsigned spins = 0; spins < SPIN_CAP; ++spins) {
                REREAD();
#pragma unroll
                for (int e = 0; e < 8; ++e)
                    if ((u32)(__ballot(a[e][1] != TAG) >> (16 * q)) << 16) a[e] = __builtin_amdgcn_raw_buffer_load_b64(rs, lo2[e], SO2(e), SC1);
                bad = 0;
#pragma unroll
                for (int e = 0; e < 8; ++e) bad |= a[e][1] ^ TAG;
                KEEP(bad);
                if (__all(bad == 0)) break;
            }
        }
        u32x4 d[2];
#pragma unroll
        for (int aa = 0; aa < 2; ++aa) {
            const u32x4 f = {a[4 * aa][0], a[4 * aa + 1][0], a[4 * aa + 2][0], a[4 * aa + 3][0]};
            d[aa] = pk4(mma(M1r[aa], f, z4), mma(M1i[aa], f, z4));
        }
#pragma unroll
        for (int i = 0; i < 4; ++i) *(LDSP u32x2*)(sm + L_X1 + xw + i * 1024) = u32x2{d[0][i], d[1][i]};
        BAR();
        f32x4 Xr[2], Xi[2];
#pragma unroll
        for (int uu = 0; uu < 2; ++uu) {
            const u32x4 f = *(const LDSP u32x4*)(sm + L_X1 + (2 * wv + uu) * 1024 + lane * 16);
            Xr[uu] = mma(F2r, f, z4); Xi[uu] = mma(F2i, f, z4);
        }
#pragma unroll
        for (int f = 0; f < 2; ++f) {
            f32x4 er[2], ei[2];
#pragma unroll
            for (int uu = 0; uu < 2; ++uu) {
                f32x4 yr, yi;
                cmul<1>(Xr[uu], Xi[uu], W[4 * f + uu], W[4 * f + 2 + uu], yr, yi);
                const u32x4 y = pk4(yr, yi);
                er[uu] = mma(G1r, y, z4); ei[uu] = mma(G1i, y, z4);
            }
            if (f == 0) { PIN(); WSTAGE(4, 8); }
            const u32x4 e0 = pk4(er[0], ei[0]), e1 = pk4(er[1], ei[1]);
#pragma unroll
            for (int i = 0; i < 4; ++i) *(LDSP u32x2*)(sm + (f ? xw2b : xw2a) + i * 1024) = u32x2{e0[i], e1[i]};
        }
        BAR();
        u32x4 fx[4];
#pragma unroll
        for (int j = 0; j < 4; ++j) fx[j] = *(const LDSP u32x4*)(sm + x2r + (j & 1) * 16384 + (j >> 1) * 1024);
        PIN();
        f32x4 qr[4], qi[4];
#pragma unroll
        for (int j = 0; j < 4; ++j) { qr[j] = mma(M2r[j >> 1], fx[j], z4); qi[j] = mma(M2i[j >> 1], fx[j], z4); if (j == 2) PIN(); }
#pragma unroll
        for (int e = 0; e < 8; ++e)
            __builtin_amdgcn_raw_buffer_store_b64(u32x2{pk(qr[e >> 2 << 1][e & 3], qi[e >> 2 << 1][e & 3]), pk(qr[(e >> 2 << 1) + 1][e & 3], qi[(e >> 2 << 1) + 1][e & 3])},
                                                  rs, lo2[e], SO2(e), SC1);
    }
    u32x4 Fr[2][2], Fi[2][2], Er_[2], Ei_[2], Mix;
    {
        u32 fr[4], fi[4];
#pragma unroll
        for (int i = 0; i < 4; ++i) { fr[i] = pk(c4[i], -s4[i]); fi[i] = pk(s4[i], c4[i]); }
#pragma unroll
        for (int Tp = 0; Tp < 2; ++Tp)
#pragma unroll
            for (int T = 0; T < 2; ++T) {
                const u32 m0 = T ? mn : 0u, m1 = m0 ^ (Tp ? NEG2 : 0u);
                Fr[Tp][T] = u32x4{fr[0] ^ m0, fr[1] ^ m1, fr[2] ^ m0, fr[3] ^ m1};
                Fi[Tp][T] = u32x4{fi[0] ^ m0, fi[1] ^ m1, fi[2] ^ m0, fi[3] ^ m1};
                KEEP(Fr[Tp][T]); KEEP(Fi[Tp][T]);
            }
#pragma unroll
        for (int s = 0; s < 2; ++s) {
            const u32 m0 = s ? mn : 0u, m1 = m0 ^ (h ? NEG2 : 0u);
            Er_[s] = u32x4{fr[0] ^ m0, fr[1] ^ m1, fr[2] ^ m0, fr[3] ^ m1};
            Ei_[s] = u32x4{fi[0] ^ m0, fi[1] ^ m1, fi[2] ^ m0, fi[3] ^ m1};
            KEEP(Er_[s]); KEEP(Ei_[s]);
        }
        const f32x4 G = *(const LDSP f32x4*)(sm + L_G + (n >> 2) * 16);
        const int pp = n >> 2, i = n & 3;
        const float sc = 1.0f / 8192.0f;
#pragma unroll
        for (int rr = 0; rr < 2; ++rr) {
            const float a = (float)(pp * (2 * q + rr)) * 0.125f;
            const float cc = cosr(a) * sc, ss = sinr(a) * sc;
            const float tau = ((rr == 0) == (i < 2)) ? 1.f : -1.f;
#pragma unroll
            for (int f = 0; f < 2; ++f) {
                const float g0 = G[2 * f], g1 = tau * G[2 * f + 1];
                const float cr = cc * g0 - ss * g1, ci = ss * g0 + cc * g1;
                Mix[2 * rr + f] = (i & 1) ? (i == 1 ? pk(ci, cr) : pk(-ci, -cr)) : pk(cr, -ci);
            }
        }
        KEEP(Mix);
    }
    int ow = L_XS + p * 2048 + (n >> 1) * 128 + (((4 * (n & 1) + 2 * h + (q >> 1)) ^ (n >> 1)) << 4) + (q & 1) * 8;
    int og = (s1 + 256 * wv) * 2048 + ((lane ^ (lane >> 3)) << 4);
    int zr0 = L_ZS + p * ZP + n * 128 + ((q ^ (n & 7)) << 4), zr1 = zr0 ^ 64;
    int vx = wv == 6 ? xpos + q * 4096 : 0x40000000;
    float dcw = (wv == 0 && n == 0) ? 0.5f : 1.0f, dcx = (wv == 0 && n == 0) ? 0.5f : 0.0f;
    KEEP(dcw); KEEP(dcx);
    int dcl = tid == 448 ? DPOS(s1) : 0x40000000;
    KEEP(dcl);
    KEEP(vx); KEEP(ow); KEEP(og); KEEP(zr0); KEEP(zr1);
    PIN();
    asm volatile("s_waitcnt vmcnt(0)" ::: "memory");
    __builtin_amdgcn_s_sleep(8);
    PIN();
    {
        u32x2 a[8], b0, b1;
#define L2LOAD() do { \
        b0 = __builtin_amdgcn_raw_buffer_load_b64(rs, vx, 0, SC1); b1 = __builtin_amdgcn_raw_buffer_load_b64(rs, vx, 2048, SC1); \
        _Pragma("unroll") for (int e = 0; e < 8; ++e) a[e] = __builtin_amdgcn_raw_buffer_load_b64(rs, lo1, SO1(e), SC1); } while (0)
#define L2MIN(m) do { m = min(b0[1] ^ TAG, b1[1] ^ TAG); \
        _Pragma("unroll") for (int e = 0; e < 8; e += 2) m = min(m, min(a[e][1] ^ TAG, a[e + 1][1] ^ TAG)); KEEP(m); } while (0)
        L2LOAD();
        u32 fresh;
        L2MIN(fresh);
        if (__any(fresh == 0u)) {
            for (unsigned spins = 0; spins < SPIN_CAP; ++spins) {
                REREAD();
                if (b0[1] == TAG) b0 = __builtin_amdgcn_raw_buffer_load_b64(rs, vx, 0, SC1);
                if (b1[1] == TAG) b1 = __builtin_amdgcn_raw_buffer_load_b64(rs, vx, 2048, SC1);
#pragma unroll
                for (int e = 0; e < 8; ++e)
                    if (a[e][1] == TAG) a[e] = __builtin_amdgcn_raw_buffer_load_b64(rs, lo1, SO1(e), SC1);
                L2MIN(fresh);
                if (!__any(fresh == 0u)) break;
            }
        }
        LDSP char* zq = sm + L_ZS + q * ZP;
        u32x4 dv, mv;
        f32x4 d[4];
#pragma unroll
        for (int e = 0; e < 4; ++e) d[e] = mma(Mix, u32x4{a[2 * e][0], a[2 * e][1], a[2 * e + 1][0], a[2 * e + 1][1]}, z4);
        PIN();
        d[0][0] = d[0][0] * dcw + d[0][2] * dcx; d[0][1] = d[0][1] * dcw + d[0][3] * dcx;
#pragma unroll
        for (int e = 0; e < 4; ++e) { dv[e] = pk(d[e][0], d[e][1]); mv[3 - e] = pk(d[e][2], d[e][3]); }
        *(LDSP u32x4*)(sm + zdo) = dv;
        *(LDSP u32x4*)(sm + zmo) = mv;
        if (rho == 0) {
#pragma unroll
            for (int e = 0; e < 4; ++e) {
                const int kk = 32 - 4 * p - e;
                if (kk < 32) *(LDSP u32*)(zq + kk * 4) = mv[3 - e];
            }
        }
        if (wv == 6) {
            const f32x4 d = mma(Mix, u32x4{b0[0], b0[1], b1[0], b1[1]}, z4);
            if (n == 0) *(LDSP u32*)(zq + 64) = pk(0.5f * (d[0] + d[2]), 0.5f * (d[1] + d[3]));
        }
    }
    BAR();
    __builtin_amdgcn_raw_buffer_store_b64(u32x2{0u, 0u}, rs, dcl, 0, SC1);
    {
        u32x4 za[2][2];
#pragma unroll
        for (int T = 0; T < 2; ++T) { za[T][0] = *(const LDSP u32x4*)(sm + zr0 + T * 2048); za[T][1] = *(const LDSP u32x4*)(sm + zr1 + T * 2048); }
        PIN();
        f32x4 er[2], ei[2], br, bi, orr[2], oi[2];
#pragma unroll
        for (int T = 0; T < 2; ++T) {
            er[T] = mma(za[T][0], Er_[0], z4); ei[T] = mma(za[T][0], Ei_[0], z4);
            er[T] = mma(za[T][1], Er_[1], er[T]); ei[T] = mma(za[T][1], Ei_[1], ei[T]);
            PIN();
        }
        cmul<1>(er[0], ei[0], tc[0], ts[0], br, bi);
        const u32x4 Ad0 = pk4(br, bi);
        PIN();
#pragma unroll
        for (int Tp = 0; Tp < 2; ++Tp) { orr[Tp] = mma(Ad0, Fr[Tp][0], z4); oi[Tp] = mma(Ad0, Fi[Tp][0], z4); }
        cmul<1>(er[1], ei[1], tc[1], ts[1], br, bi);
        const u32x4 Ad1 = pk4(br, bi);
        PIN();
#pragma unroll
        for (int Tp = 0; Tp < 2; ++Tp) { orr[Tp] = mma(Ad1, Fr[Tp][1], orr[Tp]); oi[Tp] = mma(Ad1, Fi[Tp][1], oi[Tp]); }
        PIN();
        const __amdgpu_buffer_rsrc_t ro = __builtin_amdgcn_make_buffer_rsrc(out, 0, 2048 * 1024 * 2, 0x00020000);
#pragma unroll
        for (int Tp = 0; Tp < 2; ++Tp) {
            *(LDSP u32x2*)(sm + ow + 1024 * Tp) = u32x2{pk(orr[Tp][0], orr[Tp][1]), pk(orr[Tp][2], orr[Tp][3])};
            *(LDSP u32x2*)(sm + ow + 8192 + 1024 * Tp) = u32x2{pk(oi[Tp][0], oi[Tp][1]), pk(oi[Tp][2], oi[Tp][3])};
        }
        BAR();
        const u32x4 o0 = *(const LDSP u32x4*)(sm + L_XS + wv * 2048 + lane * 16), o1 = *(const LDSP u32x4*)(sm + L_XS + wv * 2048 + 1024 + lane * 16);
        __builtin_amdgcn_raw_buffer_store_b128(o0, ro, og, 0, SC1);
        __builtin_amdgcn_raw_buffer_store_b128(o1, ro, og + 1024, 0, SC1);
    }
}

extern "C" void kernel_launch(void* const* d_in, const int* in_sizes, int n_in,
                              void* d_out, int out_size, void* d_ws, size_t ws_size,
                              hipStream_t stream) {
    gf_fused<<<256, 576, 0, stream>>>((const u16*)d_in[0], (const u16*)d_in[6], (const u16*)d_in[1], (const u16*)d_in[2],
                                      (const u16*)d_in[4], (const u16*)d_in[5], (char*)d_ws, (u16*)d_out);
}
```
